```python
import torch

def _softmax(x, axis=-1):
    return torch.softmax(x, dim=axis)

def _gelu(x):
    return torch.nn.functional.gelu(x, approximate='tanh')

def _silu(x):
    return torch.nn.functional.silu(x)

def _sigmoid(x):
    return torch.sigmoid(x)

def _layernorm(x, axis=-1, eps=1e-05):
    m = x.mean(dim=axis, keepdim=True)
    v = x.var(dim=axis, keepdim=True, unbiased=False)
    return (x - m) / torch.sqrt(v + eps)

def _scatter_add(tgt, idx, src, axis=0):
    return tgt.clone().index_add_(axis, idx, src)

def _segment_sum(src, seg, n):
    out = torch.zeros((n,) + src.shape[1:], dtype=src.dtype)
    return out.index_add_(0, seg, src)

def _pad(x, pw):
    flat = tuple((v for pair in reversed(pw) for v in pair))
    return torch.nn.functional.pad(x, flat)
kernel_name = 'standalone__autoregressive_conv_wavernn__speech'

def setup_inputs():

    _w = lambda a: (lambda t: t.to(torch.bfloat16) if t.is_floating_point() else t)(torch.as_tensor(a))
    B, T, D, K = (2, 1000, 512, 15)
    return {'x': _w((lambda *s: torch.randn(*s, dtype=torch.bfloat16) * 0.05)(B, T, D)), 'w': _w((lambda *s: torch.randn(*s, dtype=torch.bfloat16) * 0.05)(K, D, 2 * D))}

def reference(x, w):
    B, T, D = x.shape
    K = w.shape[0]
    dil = 2
    pad = (K - 1) * dil
    xp = _pad(x, ((0, 0), (pad, 0), (0, 0)))
    h = torch.einsum('btd,de->bte', xp[:, 0:T, :], w[0])
    for i in range(1, K):
        h = h + torch.einsum('btd,de->bte', xp[:, i * dil:i * dil + T, :], w[i])
    f, g = (h[..., :D].clone(), h[..., D:].clone())
    return torch.tanh(f) * _sigmoid(g)
```

```cpp
#include <hip/hip_runtime.h>
#include <hip/hip_bf16.h>

__global__ __launch_bounds__(256) __attribute__((aligned(4096))) void conv_wavernn_kernel(const char* __restrict__ x, const char* __restrict__ w,
                                                           char* __restrict__ out, char* __restrict__ ws) {
  __shared__ __attribute__((aligned(256))) char lds[163840];
  register unsigned tid asm("v0") = threadIdx.x;
  const unsigned bid = blockIdx.x;
  const unsigned lds0 = (unsigned)(size_t)lds;
  auto ka = __builtin_amdgcn_kernarg_segment_ptr();
  asm volatile(
    "L_beg_%=:\n"
    "s_load_dwordx2 s[44:45], %[ka], 0x8\n"
    "s_load_dwordx2 s[40:41], %[ka], 0x0\n"
    "s_load_dwordx4 s[36:39], %[ka], 0x10\n"
    "s_getpc_b64 s[98:99]\n"
    "L_pc_%=:\n"
    "v_and_b32 v44, 7, v0\n"
    "s_and_b32 s90, %[bid], 0xf8\n"
    "v_add_lshl_u32 v44, v44, s90, 7\n"
    "v_min_u32 v44, L_fin_%=-L_pc_%=-4, v44\n"
    "global_load_dword v23, v44, s[98:99]\n"
    "v_readfirstlane_b32 s88, v0\n"
    "v_lshrrev_b32 v40, 1, v0\n"
    "v_and_b32 v0, 63, v0\n"
    "s_bfe_u32 s91, %[bid], 0x30004\n"
    "s_lshl_b32 s91, s91, 17\n"
    "s_and_b32 s90, %[bid], 8\n"
    "s_lshl4_add_u32 s90, s90, s91\n"
    "v_lshl_add_u32 v40, v40, 10, s90\n"
    "s_lshr_b32 s88, s88, 6\n"
    "s_and_b32 s89, %[bid], 15\n"
    "s_lshl_b32 s89, s89, 7\n"
    "s_lshl_b32 s90, s88, 16\n"
    "s_add_u32 s89, s89, s90\n"
    "s_lshr_b32 s96, %[bid], 5\n"
    "s_lshl_b32 s96, s96, 20\n"
    "s_bfe_u32 s97, %[bid], 0x10004\n"
    "s_lshl_b32 s97, s97, 15\n"
    "s_add_u32 s96, s96, s97\n"
    "s_add_u32 s96, s96, s89\n"
    "v_and_b32 v46, 15, v0\n"
    "v_lshl_add_u32 v22, v46, 11, s96\n"
    "s_mov_b32 s46, 15728640\n"
    "s_mov_b32 s47, 0x00020000\n"
    "s_mov_b32 s42, 1024000\n"
    "s_mov_b32 s43, 0x00020000\n"
    "s_mov_b32 s52, 0\n"
    "s_mov_b32 s59, 7340032\n"
    "s_lshr_b32 s93, %[bid], 7\n"
    "s_mul_i32 s93, s93, 1024000\n"
    "s_waitcnt lgkmcnt(0)\n"
    "s_add_u32 s40, s40, s93\n"
    "s_addc_u32 s41, s41, 0\n"
    "buffer_load_dword v23, v40, s[40:43], 0 offen offset:0\n"
    "buffer_load_dword v23, v22, s[44:47], s52 offen\n"
    "buffer_load_dword v23, v22, s[44:47], s59 offen\n"
    "s_mul_i32 s75, s88, 40960\n"
    "s_add_u32 s75, s75, %[lds]\n"
    "v_lshrrev_b32 v44, 3, v0\n"
    "v_and_b32 v45, 2, v44\n"
    "v_xor_b32 v45, v45, v0\n"
    "v_and_b32 v45, 7, v45\n"
    "v_lshlrev_b32 v45, 4, v45\n"
    "v_lshl_add_u32 v45, v44, 11, v45\n"
    "v_add_u32 v1, s89, v45\n"
    "v_xor_b32 v2, 64, v1\n"
    "v_add_u32 v2, 15360, v2\n"
    "v_add_u32 v3, 30720, v1\n"
    "v_add_u32 v4, 30720, v2\n"
    "s_mov_b32 s53, 1048576\n"
    "s_mov_b32 s54, 2097152\n"
    "s_mov_b32 s55, 3145728\n"
    "s_mov_b32 s56, 4194304\n"
    "s_mov_b32 s57, 5242880\n"
    "s_mov_b32 s58, 6291456\n"
    "s_mov_b32 s60, 8388608\n"
    "s_mov_b32 s61, 9437184\n"
    "s_mov_b32 s62, 10485760\n"
    "s_mov_b32 s63, 11534336\n"
    "s_mov_b32 s64, 12582912\n"
    "s_mov_b32 s65, 13631488\n"
    "s_mov_b32 s66, 14680064\n"
    "s_add_u32 s68, s75, 0\n"
    "s_add_u32 s69, s75, 4096\n"
    "s_add_u32 s70, s75, 8192\n"
    "s_add_u32 s71, s75, 12288\n"
    "s_add_u32 s72, s75, 16384\n"
    "s_add_u32 s73, s75, 20480\n"
    "s_add_u32 s74, s75, 24576\n"
    "s_lshl_b32 s92, s88, 6\n"
    "s_add_u32 s92, s92, s91\n"
    "s_sub_u32 s92, s92, 28672\n"
    "v_bfe_i32 v45, v0, 4, 1\n"
    "v_xor_b32 v45, v45, v0\n"
    "v_and_b32 v45, 3, v45\n"
    "v_lshlrev_b32 v45, 4, v45\n"
    "v_lshl_add_u32 v45, v44, 14, v45\n"
    "v_and_b32 v46, 4, v0\n"
    "v_lshl_add_u32 v45, v46, 8, v45\n"
    "v_add_u32 v12, s92, v45\n"
    "v_xor_b32 v46, 48, v12\n"
    "v_add_u32 v13, 0xffff8800, v46\n"
    "v_add_u32 v14, 0xffff1000, v12\n"
    "v_add_u32 v15, 0xfffe9800, v46\n"
    "v_add_u32 v16, 0x9800, v46\n"
    "s_mov_b32 exec_hi, 0xffff\n"
    "v_add_u32 v15, 0x11000, v12\n"
    "s_mov_b32 exec_hi, 0\n"
    "v_add_u32 v14, 0x18800, v46\n"
    "s_mov_b32 exec_lo, 0xffff\n"
    "v_add_u32 v13, 0x20000, v12\n"
    "s_mov_b64 exec, -1\n"
    "v_add_u32 v17, 8192, v12\n"
    "v_add_u32 v18, 8192, v13\n"
    "v_add_u32 v19, 8192, v14\n"
    "v_add_u32 v20, 8192, v15\n"
    "v_add_u32 v21, 8192, v16\n"
    "s_mov_b32 m0, s68\n"
    "s_nop 0\n"
    "buffer_load_dwordx4 v1, s[44:47], s52 offen offset:0 lds\n"
    "buffer_load_dwordx4 v2, s[44:47], s52 offen offset:1024 lds\n"
    "buffer_load_dwordx4 v3, s[44:47], s52 offen offset:2048 lds\n"
    "buffer_load_dwordx4 v4, s[44:47], s52 offen offset:3072 lds\n"
    "s_add_u32 m0, s75, 28672\n"
    "s_nop 0\n"
    "buffer_load_dwordx4 v12, s[40:43], 0 offen lds\n"
    "s_bfe_u32 s94, %[bid], 0x10003\n"
    "v_lshrrev_b32 v45, 4, v0\n"
    "v_bfe_u32 v46, v0, 2, 2\n"
    "v_and_b32 v47, 3, v44\n"
    "s_lshl_b32 s95, s94, 1\n"
    "v_xor_b32 v47, s95, v47\n"
    "v_and_b32 v48, 3, v0\n"
    "v_lshlrev_b32 v48, 3, v48\n"
    "v_lshl_add_u32 v48, v47, 5, v48\n"
    "v_lshl_add_u32 v48, v46, 7, v48\n"
    "v_lshl_add_u32 v48, v45, 10, v48\n"
    "v_add_u32 v5, s75, v48\n"
    "v_xor_b32 v6, 32, v5\n"
    "v_xor_b32 v7, 64, v5\n"
    "v_xor_b32 v8, 96, v5\n"
    "v_bfe_u32 v46, v0, 1, 3\n"
    "v_bfe_i32 v47, v46, 1, 1\n"
    "v_and_b32 v47, 3, v47\n"
    "v_xor_b32 v47, v47, v45\n"
    "v_and_b32 v48, 1, v0\n"
    "v_lshl_add_u32 v47, v48, 2, v47\n"
    "v_lshlrev_b32 v47, 4, v47\n"
    "v_lshl_add_u32 v47, v46, 7, v47\n"
    "v_add_u32 v9, s75, v47\n"
    "v_bfe_u32 v46, v0, 1, 3\n"
    "v_add_u32 v46, 1, v46\n"
    "v_bfe_i32 v47, v46, 1, 1\n"
    "v_and_b32 v47, 3, v47\n"
    "v_xor_b32 v47, v47, v45\n"
    "v_and_b32 v48, 1, v0\n"
    "v_lshl_add_u32 v47, v48, 2, v47\n"
    "v_lshlrev_b32 v47, 4, v47\n"
    "v_lshl_add_u32 v47, v46, 7, v47\n"
    "v_add_u32 v10, s75, v47\n"
    "v_xor_b32 v11, 48, v9\n"
    "v_and_b32 v46, 15, v0\n"
    "v_lshlrev_b32 v24, 4, v0\n"
    "v_lshlrev_b32 v25, 2, v0\n"
    "s_lshl_b32 s96, %[bid], 2\n"
    "s_add_u32 s96, s96, s88\n"
    "s_xor_b32 s97, s96, 32\n"
    "s_lshl_b32 s90, s96, 12\n"
    "s_add_u32 s76, s38, s90\n"
    "s_addc_u32 s77, s39, 0\n"
    "s_lshl_b32 s90, s97, 12\n"
    "s_add_u32 s78, s38, s90\n"
    "s_addc_u32 s79, s39, 0\n"
    "s_lshl_b32 s90, s96, 8\n"
    "s_add_u32 s90, s90, 4194304\n"
    "s_add_u32 s80, s38, s90\n"
    "s_addc_u32 s81, s39, 0\n"
    "s_lshl_b32 s90, s97, 8\n"
    "s_add_u32 s90, s90, 4194304\n"
    "s_add_u32 s82, s38, s90\n"
    "s_addc_u32 s83, s39, 0\n"
    "s_add_u32 s48, s36, s93\n"
    "s_addc_u32 s49, s37, 0\n"
    "s_mov_b32 s50, 1024000\n"
    "s_mov_b32 s51, 0x00020000\n"
    "s_cmp_eq_u32 s94, 0\n"
    "s_mov_b32 s84, 0xbfb8aa3b\n"
    "s_cselect_b32 s84, 0x4038aa3b, s84\n"
    "s_cselect_b32 s85, -2.0, 1.0\n"
    "s_cselect_b32 s86, 1.0, 0\n"
    "v_mov_b32 v34, s86\n"
    "v_lshlrev_b32 v47, 2, v45\n"
    "v_xor_b32 v47, v47, v46\n"
    "v_lshlrev_b32 v47, 4, v47\n"
    "v_lshl_add_u32 v47, v45, 8, v47\n"
    "v_add_u32 v28, s75, v47\n"
    "v_and_b32 v45, 3, v0\n"
    "s_and_b32 s90, s88, 1\n"
    "s_lshl_b32 s95, s90, 3\n"
    "v_add_u32 v46, s95, v44\n"
    "v_lshlrev_b32 v47, 2, v45\n"
    "v_xor_b32 v47, v47, v46\n"
    "v_lshlrev_b32 v47, 4, v47\n"
    "v_and_b32 v48, 7, v0\n"
    "v_lshl_add_u32 v47, v48, 8, v47\n"
    "s_lshr_b32 s95, s88, 1\n"
    "s_lshl_b32 s97, s95, 11\n"
    "s_add_u32 s97, s97, %[lds]\n"
    "v_add_u32 v29, s97, v47\n"
    "v_add_u32 v30, 40960, v29\n"
    "v_add_u32 v31, 81920, v29\n"
    "v_add_u32 v32, 122880, v29\n"
    "s_lshl_b32 s95, s95, 11\n"
    "s_lshl_b32 s90, s90, 16\n"
    "s_add_u32 s95, s95, s90\n"
    "s_add_u32 s95, s95, s91\n"
    "s_and_b32 s90, %[bid], 7\n"
    "s_lshl_b32 s90, s90, 7\n"
    "s_add_u32 s95, s95, s90\n"
    "s_xor_b32 s90, s94, 1\n"
    "s_lshl_b32 s90, s90, 6\n"
    "s_add_u32 s95, s95, s90\n"
    "v_lshlrev_b32 v48, 3, v48\n"
    "v_and_b32 v47, 1, v44\n"
    "v_lshl_add_u32 v48, v47, 10, v48\n"
    "v_lshrrev_b32 v47, 1, v44\n"
    "v_lshl_add_u32 v48, v47, 14, v48\n"
    "v_add_u32 v33, s95, v48\n"
    "s_mov_b64 exec, 0\n"
    "s_cmp_eq_u32 s88, 0\n"
    "s_cbranch_scc1 L_st1_%=\n"
    "s_cmp_eq_u32 s88, 1\n"
    "s_cbranch_scc1 L_st2_%=\n"
    "s_cmp_eq_u32 s88, 2\n"
    "s_cbranch_scc1 L_st3_%=\n"
    "s_branch L_st4_%=\n"
    "L_back_%=:\n"
    "s_mov_b64 exec, -1\n"
    "s_add_u32 m0, s75, 29696\n"
    "s_nop 0\n"
    "buffer_load_dwordx4 v13, s[40:43], 0 offen lds\n"
    "s_add_u32 m0, s75, 30720\n"
    "s_nop 0\n"
    "buffer_load_dwordx4 v14, s[40:43], 0 offen lds\n"
    "s_mov_b32 m0, s69\n"
    "s_nop 0\n"
    "buffer_load_dwordx4 v1, s[44:47], s53 offen offset:0 lds\n"
    "buffer_load_dwordx4 v2, s[44:47], s53 offen offset:1024 lds\n"
    "buffer_load_dwordx4 v3, s[44:47], s53 offen offset:2048 lds\n"
    "buffer_load_dwordx4 v4, s[44:47], s53 offen offset:3072 lds\n"
    "s_add_u32 m0, s75, 31744\n"
    "s_nop 0\n"
    "buffer_load_dwordx4 v15, s[40:43], 0 offen lds\n"
    "s_waitcnt vmcnt(8)\n"
    "ds_read_b64_tr_b16 v[44:45], v5 offset:0\n"
    "ds_read_b64_tr_b16 v[46:47], v5 offset:512\n"
    "s_add_u32 m0, s75, 32768\n"
    "s_nop 0\n"
    "buffer_load_dwordx4 v16, s[40:43], 0 offen lds\n"
    "s_waitcnt vmcnt(8)\n"
    "ds_read_b128 a[128:131], v9 offset:28672\n"
    "ds_read_b64_tr_b16 v[48:49], v6 offset:0\n"
    "ds_read_b64_tr_b16 v[50:51], v6 offset:512\n"
    "ds_read_b64_tr_b16 v[52:53], v7 offset:0\n"
    "ds_read_b64_tr_b16 v[54:55], v7 offset:512\n"
    "ds_read_b64_tr_b16 v[56:57], v8 offset:0\n"
    "ds_read_b64_tr_b16 v[58:59], v8 offset:512\n"
    "s_add_u32 m0, s75, 33792\n"
    "s_nop 0\n"
    "buffer_load_dwordx4 v17, s[40:43], 0 offen lds\n"
    "s_mov_b32 m0, s70\n"
    "s_nop 0\n"
    "buffer_load_dwordx4 v1, s[44:47], s54 offen offset:0 lds\n"
    "s_waitcnt vmcnt(8)\n"
    "ds_read_b128 a[132:135], v9 offset:29952\n"
    "buffer_load_dwordx4 v2, s[44:47], s54 offen offset:1024 lds\n"
    "buffer_load_dwordx4 v3, s[44:47], s54 offen offset:2048 lds\n"
    "buffer_load_dwordx4 v4, s[44:47], s54 offen offset:3072 lds\n"
    "s_add_u32 m0, s75, 34816\n"
    "s_nop 0\n"
    "buffer_load_dwordx4 v18, s[40:43], 0 offen lds\n"
    "s_waitcnt vmcnt(8)\n"
    "ds_read_b64_tr_b16 v[60:61], v5 offset:4096\n"
    "ds_read_b64_tr_b16 v[62:63], v5 offset:4608\n"
    "ds_read_b64_tr_b16 v[64:65], v6 offset:4096\n"
    "ds_read_b64_tr_b16 v[66:67], v6 offset:4608\n"
    "ds_read_b64_tr_b16 v[68:69], v7 offset:4096\n"
    "ds_read_b64_tr_b16 v[70:71], v7 offset:4608\n"
    "s_waitcnt lgkmcnt(13)\n"
    "v_mfma_f32_16x16x32_bf16 a[0:3], v[44:47], a[128:131], 0\n"
    "ds_read_b64_tr_b16 v[72:73], v8 offset:4096\n"
    "ds_read_b64_tr_b16 v[74:75], v8 offset:4608\n"
    "s_waitcnt lgkmcnt(13)\n"
    "v_mfma_f32_16x16x32_bf16 a[4:7], v[48:51], a[128:131], 0\n"
    "s_waitcnt lgkmcnt(11)\n"
    "v_mfma_f32_16x16x32_bf16 a[8:11], v[52:55], a[128:131], 0\n"
    "s_add_u32 m0, s75, 35840\n"
    "s_waitcnt lgkmcnt(9)\n"
    "v_mfma_f32_16x16x32_bf16 a[12:15], v[56:59], a[128:131], 0\n"
    "buffer_load_dwordx4 v19, s[40:43], 0 offen lds\n"
    "s_waitcnt lgkmcnt(8)\n"
    "v_mfma_f32_16x16x32_bf16 a[16:19], v[44:47], a[132:135], 0\n"
    "ds_read_b128 a[136:139], v10 offset:28672\n"
    "v_mfma_f32_16x16x32_bf16 a[20:23], v[48:51], a[132:135], 0\n"
    "v_mfma_f32_16x16x32_bf16 a[24:27], v[52:55], a[132:135], 0\n"
    "s_add_u32 m0, s75, 36864\n"
    "v_mfma_f32_16x16x32_bf16 a[28:31], v[56:59], a[132:135], 0\n"
    "buffer_load_dwordx4 v20, s[40:43], 0 offen lds\n"
    "s_waitcnt lgkmcnt(1)\n"
    "v_mfma_f32_16x16x32_bf16 a[0:3], v[60:63], a[132:135], a[0:3]\n"
    "s_waitcnt vmcnt(9)\n"
    "ds_read_b128 a[140:143], v9 offset:31232\n"
    "v_mfma_f32_16x16x32_bf16 a[4:7], v[64:67], a[132:135], a[4:7]\n"
    "v_mfma_f32_16x16x32_bf16 a[8:11], v[68:71], a[132:135], a[8:11]\n"
    "s_add_u32 m0, s75, 37888\n"
    "v_mfma_f32_16x16x32_bf16 a[12:15], v[72:75], a[132:135], a[12:15]\n"
    "buffer_load_dwordx4 v21, s[40:43], 0 offen lds\n"
    "s_waitcnt lgkmcnt(1)\n"
    "v_mfma_f32_16x16x32_bf16 a[112:115], v[60:63], a[136:139], 0\n"
    "v_mfma_f32_16x16x32_bf16 a[116:119], v[64:67], a[136:139], 0\n"
    "v_mfma_f32_16x16x32_bf16 a[120:123], v[68:71], a[136:139], 0\n"
    "s_mov_b32 m0, s71\n"
    "v_mfma_f32_16x16x32_bf16 a[124:127], v[72:75], a[136:139], 0\n"
    "buffer_load_dwordx4 v1, s[44:47], s55 offen offset:0 lds\n"
    "s_waitcnt lgkmcnt(0)\n"
    "v_mfma_f32_16x16x32_bf16 a[32:35], v[44:47], a[140:143], 0\n"
    "s_waitcnt vmcnt(10)\n"
    "ds_read_b128 a[144:147], v9 offset:32512\n"
    "v_mfma_f32_16x16x32_bf16 a[36:39], v[48:51], a[140:143], 0\n"
    "v_mfma_f32_16x16x32_bf16 a[40:43], v[52:55], a[140:143], 0\n"
    "v_mfma_f32_16x16x32_bf16 a[44:47], v[56:59], a[140:143], 0\n"
    "buffer_load_dwordx4 v2, s[44:47], s55 offen offset:1024 lds\n"
    "v_mfma_f32_16x16x32_bf16 a[16:19], v[60:63], a[140:143], a[16:19]\n"
    "v_mfma_f32_16x16x32_bf16 a[20:23], v[64:67], a[140:143], a[20:23]\n"
    "v_mfma_f32_16x16x32_bf16 a[24:27], v[68:71], a[140:143], a[24:27]\n"
    "v_mfma_f32_16x16x32_bf16 a[28:31], v[72:75], a[140:143], a[28:31]\n"
    "buffer_load_dwordx4 v3, s[44:47], s55 offen offset:2048 lds\n"
    "s_waitcnt lgkmcnt(0)\n"
    "v_mfma_f32_16x16x32_bf16 a[48:51], v[44:47], a[144:147], 0\n"
    "s_waitcnt vmcnt(11)\n"
    "ds_read_b128 a[148:151], v9 offset:33792\n"
    "v_mfma_f32_16x16x32_bf16 a[52:55], v[48:51], a[144:147], 0\n"
    "v_mfma_f32_16x16x32_bf16 a[56:59], v[52:55], a[144:147], 0\n"
    "v_mfma_f32_16x16x32_bf16 a[60:63], v[56:59], a[144:147], 0\n"
    "buffer_load_dwordx4 v4, s[44:47], s55 offen offset:3072 lds\n"
    "v_mfma_f32_16x16x32_bf16 a[32:35], v[60:63], a[144:147], a[32:35]\n"
    "v_mfma_f32_16x16x32_bf16 a[36:39], v[64:67], a[144:147], a[36:39]\n"
    "s_waitcnt vmcnt(8)\n"
    "ds_read_b64_tr_b16 v[76:77], v5 offset:8192\n"
    "ds_read_b64_tr_b16 v[78:79], v5 offset:8704\n"
    "v_mfma_f32_16x16x32_bf16 a[40:43], v[68:71], a[144:147], a[40:43]\n"
    "ds_read_b64_tr_b16 v[80:81], v6 offset:8192\n"
    "ds_read_b64_tr_b16 v[82:83], v6 offset:8704\n"
    "s_mov_b32 m0, s72\n"
    "v_mfma_f32_16x16x32_bf16 a[44:47], v[72:75], a[144:147], a[44:47]\n"
    "ds_read_b64_tr_b16 v[84:85], v7 offset:8192\n"
    "ds_read_b64_tr_b16 v[86:87], v7 offset:8704\n"
    "buffer_load_dwordx4 v1, s[44:47], s56 offen offset:0 lds\n"
    "s_waitcnt lgkmcnt(6)\n"
    "v_mfma_f32_16x16x32_bf16 a[64:67], v[44:47], a[148:151], 0\n"
    "ds_read_b64_tr_b16 v[88:89], v8 offset:8192\n"
    "ds_read_b64_tr_b16 v[90:91], v8 offset:8704\n"
    "v_mfma_f32_16x16x32_bf16 a[68:71], v[48:51], a[148:151], 0\n"
    "v_mfma_f32_16x16x32_bf16 a[72:75], v[52:55], a[148:151], 0\n"
    "v_mfma_f32_16x16x32_bf16 a[76:79], v[56:59], a[148:151], 0\n"
    "buffer_load_dwordx4 v2, s[44:47], s56 offen offset:1024 lds\n"
    "v_mfma_f32_16x16x32_bf16 a[48:51], v[60:63], a[148:151], a[48:51]\n"
    "v_mfma_f32_16x16x32_bf16 a[52:55], v[64:67], a[148:151], a[52:55]\n"
    "v_mfma_f32_16x16x32_bf16 a[56:59], v[68:71], a[148:151], a[56:59]\n"
    "v_mfma_f32_16x16x32_bf16 a[60:63], v[72:75], a[148:151], a[60:63]\n"
    "buffer_load_dwordx4 v3, s[44:47], s56 offen offset:2048 lds\n"
    "s_waitcnt lgkmcnt(0)\n"
    "v_mfma_f32_16x16x32_bf16 a[0:3], v[76:79], a[140:143], a[0:3]\n"
    "v_mfma_f32_16x16x32_bf16 a[4:7], v[80:83], a[140:143], a[4:7]\n"
    "v_mfma_f32_16x16x32_bf16 a[8:11], v[84:87], a[140:143], a[8:11]\n"
    "v_mfma_f32_16x16x32_bf16 a[12:15], v[88:91], a[140:143], a[12:15]\n"
    "buffer_load_dwordx4 v4, s[44:47], s56 offen offset:3072 lds\n"
    "v_mfma_f32_16x16x32_bf16 a[16:19], v[76:79], a[144:147], a[16:19]\n"
    "v_mfma_f32_16x16x32_bf16 a[20:23], v[80:83], a[144:147], a[20:23]\n"
    "v_mfma_f32_16x16x32_bf16 a[24:27], v[84:87], a[144:147], a[24:27]\n"
    "s_mov_b32 m0, s73\n"
    "v_mfma_f32_16x16x32_bf16 a[28:31], v[88:91], a[144:147], a[28:31]\n"
    "buffer_load_dwordx4 v1, s[44:47], s57 offen offset:0 lds\n"
    "v_mfma_f32_16x16x32_bf16 a[32:35], v[76:79], a[148:151], a[32:35]\n"
    "ds_read_b128 a[152:155], v10 offset:29952\n"
    "v_mfma_f32_16x16x32_bf16 a[36:39], v[80:83], a[148:151], a[36:39]\n"
    "v_mfma_f32_16x16x32_bf16 a[40:43], v[84:87], a[148:151], a[40:43]\n"
    "v_mfma_f32_16x16x32_bf16 a[44:47], v[88:91], a[148:151], a[44:47]\n"
    "buffer_load_dwordx4 v2, s[44:47], s57 offen offset:1024 lds\n"
    "v_mfma_f32_16x16x32_bf16 a[96:99], v[76:79], a[136:139], 0\n"
    "s_waitcnt vmcnt(12)\n"
    "ds_read_b128 a[156:159], v9 offset:35072\n"
    "v_mfma_f32_16x16x32_bf16 a[100:103], v[80:83], a[136:139], 0\n"
    "v_mfma_f32_16x16x32_bf16 a[104:107], v[84:87], a[136:139], 0\n"
    "v_mfma_f32_16x16x32_bf16 a[108:111], v[88:91], a[136:139], 0\n"
    "buffer_load_dwordx4 v3, s[44:47], s57 offen offset:2048 lds\n"
    "s_waitcnt lgkmcnt(1)\n"
    "v_mfma_f32_16x16x32_bf16 a[112:115], v[76:79], a[152:155], a[112:115]\n"
    "v_mfma_f32_16x16x32_bf16 a[116:119], v[80:83], a[152:155], a[116:119]\n"
    "v_mfma_f32_16x16x32_bf16 a[120:123], v[84:87], a[152:155], a[120:123]\n"
    "v_mfma_f32_16x16x32_bf16 a[124:127], v[88:91], a[152:155], a[124:127]\n"
    "buffer_load_dwordx4 v4, s[44:47], s57 offen offset:3072 lds\n"
    "s_waitcnt lgkmcnt(0)\n"
    "v_mfma_f32_16x16x32_bf16 a[80:83], v[44:47], a[156:159], 0\n"
    "v_mfma_f32_16x16x32_bf16 a[84:87], v[48:51], a[156:159], 0\n"
    "v_mfma_f32_16x16x32_bf16 a[88:91], v[52:55], a[156:159], 0\n"
    "s_mov_b32 m0, s74\n"
    "v_mfma_f32_16x16x32_bf16 a[92:95], v[56:59], a[156:159], 0\n"
    "buffer_load_dwordx4 v1, s[44:47], s58 offen offset:0 lds\n"
    "v_mfma_f32_16x16x32_bf16 a[64:67], v[60:63], a[156:159], a[64:67]\n"
    "s_waitcnt vmcnt(14)\n"
    "ds_read_b128 a[160:163], v9 offset:36352\n"
    "v_mfma_f32_16x16x32_bf16 a[68:71], v[64:67], a[156:159], a[68:71]\n"
    "v_mfma_f32_16x16x32_bf16 a[72:75], v[68:71], a[156:159], a[72:75]\n"
    "v_mfma_f32_16x16x32_bf16 a[76:79], v[72:75], a[156:159], a[76:79]\n"
    "buffer_load_dwordx4 v2, s[44:47], s58 offen offset:1024 lds\n"
    "v_mfma_f32_16x16x32_bf16 a[48:51], v[76:79], a[156:159], a[48:51]\n"
    "v_mfma_f32_16x16x32_bf16 a[52:55], v[80:83], a[156:159], a[52:55]\n"
    "v_mfma_f32_16x16x32_bf16 a[56:59], v[84:87], a[156:159], a[56:59]\n"
    "v_mfma_f32_16x16x32_bf16 a[60:63], v[88:91], a[156:159], a[60:63]\n"
    "s_waitcnt vmcnt(10)\n"
    "ds_read_b64_tr_b16 v[92:93], v5 offset:12288\n"
    "buffer_load_dwordx4 v3, s[44:47], s58 offen offset:2048 lds\n"
    "s_waitcnt lgkmcnt(1)\n"
    "v_mfma_f32_16x16x32_bf16 a[96:99], v[44:47], a[160:163], a[96:99]\n"
    "ds_read_b64_tr_b16 v[94:95], v5 offset:12800\n"
    "v_mfma_f32_16x16x32_bf16 a[100:103], v[48:51], a[160:163], a[100:103]\n"
    "ds_read_b64_tr_b16 v[96:97], v6 offset:12288\n"
    "v_mfma_f32_16x16x32_bf16 a[104:107], v[52:55], a[160:163], a[104:107]\n"
    "ds_read_b64_tr_b16 v[98:99], v6 offset:12800\n"
    "v_mfma_f32_16x16x32_bf16 a[108:111], v[56:59], a[160:163], a[108:111]\n"
    "ds_read_b64_tr_b16 v[100:101], v7 offset:12288\n"
    "buffer_load_dwordx4 v4, s[44:47], s58 offen offset:3072 lds\n"
    "v_mfma_f32_16x16x32_bf16 a[80:83], v[60:63], a[160:163], a[80:83]\n"
    "ds_read_b64_tr_b16 v[102:103], v7 offset:12800\n"
    "ds_read_b128 a[164:167], v9 offset:37632\n"
    "v_mfma_f32_16x16x32_bf16 a[84:87], v[64:67], a[160:163], a[84:87]\n"
    "ds_read_b64_tr_b16 v[104:105], v8 offset:12288\n"
    "v_mfma_f32_16x16x32_bf16 a[88:91], v[68:71], a[160:163], a[88:91]\n"
    "ds_read_b64_tr_b16 v[106:107], v8 offset:12800\n"
    "s_mov_b32 m0, s68\n"
    "v_mfma_f32_16x16x32_bf16 a[92:95], v[72:75], a[160:163], a[92:95]\n"
    "buffer_load_dwordx4 v1, s[44:47], s59 offen offset:0 lds\n"
    "v_mfma_f32_16x16x32_bf16 a[64:67], v[76:79], a[160:163], a[64:67]\n"
    "s_waitcnt vmcnt(9)\n"
    "ds_read_b64_tr_b16 v[108:109], v5 offset:16384\n"
    "ds_read_b128 a[168:171], v10 offset:31232\n"
    "v_mfma_f32_16x16x32_bf16 a[68:71], v[80:83], a[160:163], a[68:71]\n"
    "ds_read_b64_tr_b16 v[110:111], v5 offset:16896\n"
    "v_mfma_f32_16x16x32_bf16 a[72:75], v[84:87], a[160:163], a[72:75]\n"
    "ds_read_b64_tr_b16 v[112:113], v6 offset:16384\n"
    "v_mfma_f32_16x16x32_bf16 a[76:79], v[88:91], a[160:163], a[76:79]\n"
    "ds_read_b64_tr_b16 v[114:115], v6 offset:16896\n"
    "ds_read_b128 a[172:175], v10 offset:32512\n"
    "s_waitcnt lgkmcnt(7)\n"
    "v_mfma_f32_16x16x32_bf16 a[112:115], v[44:47], a[164:167], a[112:115]\n"
    "ds_read_b64_tr_b16 v[116:117], v7 offset:16384\n"
    "buffer_load_dwordx4 v2, s[44:47], s59 offen offset:1024 lds\n"
    "v_mfma_f32_16x16x32_bf16 a[116:119], v[48:51], a[164:167], a[116:119]\n"
    "ds_read_b64_tr_b16 v[118:119], v7 offset:16896\n"
    "v_mfma_f32_16x16x32_bf16 a[120:123], v[52:55], a[164:167], a[120:123]\n"
    "ds_read_b64_tr_b16 v[120:121], v8 offset:16384\n"
    "ds_read_b128 a[176:179], v10 offset:33792\n"
    "v_mfma_f32_16x16x32_bf16 a[124:127], v[56:59], a[164:167], a[124:127]\n"
    "ds_read_b64_tr_b16 v[122:123], v8 offset:16896\n"
    "v_mfma_f32_16x16x32_bf16 a[96:99], v[60:63], a[164:167], a[96:99]\n"
    "buffer_load_dwordx4 v3, s[44:47], s59 offen offset:2048 lds\n"
    "v_mfma_f32_16x16x32_bf16 a[100:103], v[64:67], a[164:167], a[100:103]\n"
    "s_waitcnt vmcnt(7)\n"
    "ds_read_b64_tr_b16 v[124:125], v5 offset:20480\n"
    "ds_read_b128 a[180:183], v10 offset:35072\n"
    "v_mfma_f32_16x16x32_bf16 a[104:107], v[68:71], a[164:167], a[104:107]\n"
    "ds_read_b64_tr_b16 v[126:127], v5 offset:20992\n"
    "v_mfma_f32_16x16x32_bf16 a[108:111], v[72:75], a[164:167], a[108:111]\n"
    "ds_read_b64_tr_b16 v[128:129], v6 offset:20480\n"
    "v_mfma_f32_16x16x32_bf16 a[80:83], v[76:79], a[164:167], a[80:83]\n"
    "ds_read_b64_tr_b16 v[130:131], v6 offset:20992\n"
    "ds_read_b128 a[184:187], v10 offset:36352\n"
    "v_mfma_f32_16x16x32_bf16 a[84:87], v[80:83], a[164:167], a[84:87]\n"
    "ds_read_b64_tr_b16 v[132:133], v7 offset:20480\n"
    "buffer_load_dwordx4 v4, s[44:47], s59 offen offset:3072 lds\n"
    "v_mfma_f32_16x16x32_bf16 a[88:91], v[84:87], a[164:167], a[88:91]\n"
    "ds_read_b64_tr_b16 v[134:135], v7 offset:20992\n"
    "v_mfma_f32_16x16x32_bf16 a[92:95], v[88:91], a[164:167], a[92:95]\n"
    "ds_read_b64_tr_b16 v[136:137], v8 offset:20480\n"
    "ds_read_b128 a[188:191], v10 offset:37632\n"
    "v_mfma_f32_16x16x32_bf16 a[0:3], v[92:95], a[144:147], a[0:3]\n"
    "ds_read_b64_tr_b16 v[138:139], v8 offset:20992\n"
    "s_mov_b32 m0, s69\n"
    "s_waitcnt lgkmcnt(8)\n"
    "v_mfma_f32_16x16x32_bf16 a[0:3], v[108:111], a[148:151], a[0:3]\n"
    "buffer_load_dwordx4 v1, s[44:47], s60 offen offset:0 lds\n"
    "v_mfma_f32_16x16x32_bf16 a[0:3], v[124:127], a[156:159], a[0:3]\n"
    "ds_read_b128 a[192:195], v11 offset:28928\n"
    "v_mfma_f32_16x16x32_bf16 a[16:19], v[124:127], a[160:163], a[16:19]\n"
    "v_mfma_f32_16x16x32_bf16 a[16:19], v[108:111], a[156:159], a[16:19]\n"
    "v_mfma_f32_16x16x32_bf16 a[16:19], v[92:95], a[148:151], a[16:19]\n"
    "ds_read_b128 a[196:199], v11 offset:30208\n"
    "buffer_load_dwordx4 v2, s[44:47], s60 offen offset:1024 lds\n"
    "v_mfma_f32_16x16x32_bf16 a[32:35], v[92:95], a[156:159], a[32:35]\n"
    "v_mfma_f32_16x16x32_bf16 a[32:35], v[108:111], a[160:163], a[32:35]\n"
    "v_mfma_f32_16x16x32_bf16 a[32:35], v[124:127], a[164:167], a[32:35]\n"
    "ds_read_b128 a[200:203], v11 offset:31488\n"
    "v_mfma_f32_16x16x32_bf16 a[48:51], v[124:127], a[136:139], a[48:51]\n"
    "buffer_load_dwordx4 v3, s[44:47], s60 offen offset:2048 lds\n"
    "v_mfma_f32_16x16x32_bf16 a[48:51], v[108:111], a[164:167], a[48:51]\n"
    "v_mfma_f32_16x16x32_bf16 a[48:51], v[92:95], a[160:163], a[48:51]\n"
    "ds_read_b128 a[204:207], v11 offset:32768\n"
    "v_mfma_f32_16x16x32_bf16 a[64:67], v[92:95], a[164:167], a[64:67]\n"
    "v_mfma_f32_16x16x32_bf16 a[64:67], v[108:111], a[136:139], a[64:67]\n"
    "buffer_load_dwordx4 v4, s[44:47], s60 offen offset:3072 lds\n"
    "v_mfma_f32_16x16x32_bf16 a[64:67], v[124:127], a[152:155], a[64:67]\n"
    "ds_read_b128 a[208:211], v11 offset:34048\n"
    "v_mfma_f32_16x16x32_bf16 a[80:83], v[124:127], a[168:171], a[80:83]\n"
    "v_mfma_f32_16x16x32_bf16 a[80:83], v[108:111], a[152:155], a[80:83]\n"
    "s_mov_b32 m0, s70\n"
    "v_mfma_f32_16x16x32_bf16 a[80:83], v[92:95], a[136:139], a[80:83]\n"
    "ds_read_b128 a[212:215], v11 offset:35328\n"
    "buffer_load_dwordx4 v1, s[44:47], s61 offen offset:0 lds\n"
    "v_mfma_f32_16x16x32_bf16 a[96:99], v[92:95], a[152:155], a[96:99]\n"
    "v_mfma_f32_16x16x32_bf16 a[96:99], v[108:111], a[168:171], a[96:99]\n"
    "v_mfma_f32_16x16x32_bf16 a[96:99], v[124:127], a[172:175], a[96:99]\n"
    "v_mfma_f32_16x16x32_bf16 a[112:115], v[124:127], a[176:179], a[112:115]\n"
    "buffer_load_dwordx4 v2, s[44:47], s61 offen offset:1024 lds\n"
    "v_mfma_f32_16x16x32_bf16 a[112:115], v[108:111], a[172:175], a[112:115]\n"
    "v_mfma_f32_16x16x32_bf16 a[112:115], v[92:95], a[168:171], a[112:115]\n"
    "v_mfma_f32_16x16x32_bf16 a[4:7], v[96:99], a[144:147], a[4:7]\n"
    "v_mfma_f32_16x16x32_bf16 a[4:7], v[112:115], a[148:151], a[4:7]\n"
    "buffer_load_dwordx4 v3, s[44:47], s61 offen offset:2048 lds\n"
    "s_waitcnt lgkmcnt(0)\n"
    "v_mfma_f32_16x16x32_bf16 a[4:7], v[128:131], a[156:159], a[4:7]\n"
    "v_mfma_f32_16x16x32_bf16 a[20:23], v[128:131], a[160:163], a[20:23]\n"
    "v_mfma_f32_16x16x32_bf16 a[20:23], v[112:115], a[156:159], a[20:23]\n"
    "v_mfma_f32_16x16x32_bf16 a[20:23], v[96:99], a[148:151], a[20:23]\n"
    "buffer_load_dwordx4 v4, s[44:47], s61 offen offset:3072 lds\n"
    "v_mfma_f32_16x16x32_bf16 a[36:39], v[96:99], a[156:159], a[36:39]\n"
    "v_mfma_f32_16x16x32_bf16 a[36:39], v[112:115], a[160:163], a[36:39]\n"
    "v_mfma_f32_16x16x32_bf16 a[36:39], v[128:131], a[164:167], a[36:39]\n"
    "s_mov_b32 m0, s71\n"
    "v_mfma_f32_16x16x32_bf16 a[52:55], v[128:131], a[136:139], a[52:55]\n"
    "buffer_load_dwordx4 v1, s[44:47], s62 offen offset:0 lds\n"
    "v_mfma_f32_16x16x32_bf16 a[52:55], v[112:115], a[164:167], a[52:55]\n"
    "v_mfma_f32_16x16x32_bf16 a[52:55], v[96:99], a[160:163], a[52:55]\n"
    "v_mfma_f32_16x16x32_bf16 a[68:71], v[96:99], a[164:167], a[68:71]\n"
    "v_mfma_f32_16x16x32_bf16 a[68:71], v[112:115], a[136:139], a[68:71]\n"
    "buffer_load_dwordx4 v2, s[44:47], s62 offen offset:1024 lds\n"
    "v_mfma_f32_16x16x32_bf16 a[68:71], v[128:131], a[152:155], a[68:71]\n"
    "v_mfma_f32_16x16x32_bf16 a[84:87], v[128:131], a[168:171], a[84:87]\n"
    "v_mfma_f32_16x16x32_bf16 a[84:87], v[112:115], a[152:155], a[84:87]\n"
    "v_mfma_f32_16x16x32_bf16 a[84:87], v[96:99], a[136:139], a[84:87]\n"
    "buffer_load_dwordx4 v3, s[44:47], s62 offen offset:2048 lds\n"
    "v_mfma_f32_16x16x32_bf16 a[100:103], v[96:99], a[152:155], a[100:103]\n"
    "v_mfma_f32_16x16x32_bf16 a[100:103], v[112:115], a[168:171], a[100:103]\n"
    "v_mfma_f32_16x16x32_bf16 a[100:103], v[128:131], a[172:175], a[100:103]\n"
    "v_mfma_f32_16x16x32_bf16 a[116:119], v[128:131], a[176:179], a[116:119]\n"
    "buffer_load_dwordx4 v4, s[44:47], s62 offen offset:3072 lds\n"
    "v_mfma_f32_16x16x32_bf16 a[116:119], v[112:115], a[172:175], a[116:119]\n"
    "v_mfma_f32_16x16x32_bf16 a[116:119], v[96:99], a[168:171], a[116:119]\n"
    "v_mfma_f32_16x16x32_bf16 a[8:11], v[100:103], a[144:147], a[8:11]\n"
    "s_mov_b32 m0, s72\n"
    "v_mfma_f32_16x16x32_bf16 a[8:11], v[116:119], a[148:151], a[8:11]\n"
    "buffer_load_dwordx4 v1, s[44:47], s63 offen offset:0 lds\n"
    "v_mfma_f32_16x16x32_bf16 a[8:11], v[132:135], a[156:159], a[8:11]\n"
    "v_mfma_f32_16x16x32_bf16 a[24:27], v[132:135], a[160:163], a[24:27]\n"
    "v_mfma_f32_16x16x32_bf16 a[24:27], v[116:119], a[156:159], a[24:27]\n"
    "v_mfma_f32_16x16x32_bf16 a[24:27], v[100:103], a[148:151], a[24:27]\n"
    "buffer_load_dwordx4 v2, s[44:47], s63 offen offset:1024 lds\n"
    "v_mfma_f32_16x16x32_bf16 a[40:43], v[100:103], a[156:159], a[40:43]\n"
    "v_mfma_f32_16x16x32_bf16 a[40:43], v[116:119], a[160:163], a[40:43]\n"
    "v_mfma_f32_16x16x32_bf16 a[40:43], v[132:135], a[164:167], a[40:43]\n"
    "v_mfma_f32_16x16x32_bf16 a[56:59], v[132:135], a[136:139], a[56:59]\n"
    "buffer_load_dwordx4 v3, s[44:47], s63 offen offset:2048 lds\n"
    "v_mfma_f32_16x16x32_bf16 a[56:59], v[116:119], a[164:167], a[56:59]\n"
    "v_mfma_f32_16x16x32_bf16 a[56:59], v[100:103], a[160:163], a[56:59]\n"
    "v_mfma_f32_16x16x32_bf16 a[72:75], v[100:103], a[164:167], a[72:75]\n"
    "v_mfma_f32_16x16x32_bf16 a[72:75], v[116:119], a[136:139], a[72:75]\n"
    "buffer_load_dwordx4 v4, s[44:47], s63 offen offset:3072 lds\n"
    "v_mfma_f32_16x16x32_bf16 a[72:75], v[132:135], a[152:155], a[72:75]\n"
    "v_mfma_f32_16x16x32_bf16 a[88:91], v[132:135], a[168:171], a[88:91]\n"
    "v_mfma_f32_16x16x32_bf16 a[88:91], v[116:119], a[152:155], a[88:91]\n"
    "s_mov_b32 m0, s73\n"
    "v_mfma_f32_16x16x32_bf16 a[88:91], v[100:103], a[136:139], a[88:91]\n"
    "buffer_load_dwordx4 v1, s[44:47], s64 offen offset:0 lds\n"
    "v_mfma_f32_16x16x32_bf16 a[104:107], v[100:103], a[152:155], a[104:107]\n"
    "v_mfma_f32_16x16x32_bf16 a[104:107], v[116:119], a[168:171], a[104:107]\n"
    "s_waitcnt vmcnt(21)\n"
    "ds_read_b64_tr_b16 v[140:141], v5 offset:24576\n"
    "v_mfma_f32_16x16x32_bf16 a[104:107], v[132:135], a[172:175], a[104:107]\n"
    "ds_read_b64_tr_b16 v[142:143], v5 offset:25088\n"
    "v_mfma_f32_16x16x32_bf16 a[120:123], v[132:135], a[176:179], a[120:123]\n"
    "ds_read_b64_tr_b16 v[144:145], v6 offset:24576\n"
    "buffer_load_dwordx4 v2, s[44:47], s64 offen offset:1024 lds\n"
    "v_mfma_f32_16x16x32_bf16 a[120:123], v[116:119], a[172:175], a[120:123]\n"
    "ds_read_b64_tr_b16 v[146:147], v6 offset:25088\n"
    "v_mfma_f32_16x16x32_bf16 a[120:123], v[100:103], a[168:171], a[120:123]\n"
    "ds_read_b64_tr_b16 v[148:149], v7 offset:24576\n"
    "v_mfma_f32_16x16x32_bf16 a[12:15], v[104:107], a[144:147], a[12:15]\n"
    "ds_read_b64_tr_b16 v[150:151], v7 offset:25088\n"
    "v_mfma_f32_16x16x32_bf16 a[12:15], v[120:123], a[148:151], a[12:15]\n"
    "ds_read_b64_tr_b16 v[152:153], v8 offset:24576\n"
    "buffer_load_dwordx4 v3, s[44:47], s64 offen offset:2048 lds\n"
    "v_mfma_f32_16x16x32_bf16 a[12:15], v[136:139], a[156:159], a[12:15]\n"
    "ds_read_b64_tr_b16 v[154:155], v8 offset:25088\n"
    "v_mfma_f32_16x16x32_bf16 a[28:31], v[136:139], a[160:163], a[28:31]\n"
    "v_mfma_f32_16x16x32_bf16 a[28:31], v[120:123], a[156:159], a[28:31]\n"
    "v_mfma_f32_16x16x32_bf16 a[28:31], v[104:107], a[148:151], a[28:31]\n"
    "buffer_load_dwordx4 v4, s[44:47], s64 offen offset:3072 lds\n"
    "v_mfma_f32_16x16x32_bf16 a[44:47], v[104:107], a[156:159], a[44:47]\n"
    "v_mfma_f32_16x16x32_bf16 a[44:47], v[120:123], a[160:163], a[44:47]\n"
    "v_mfma_f32_16x16x32_bf16 a[44:47], v[136:139], a[164:167], a[44:47]\n"
    "s_waitcnt vmcnt(20)\n"
    "ds_read_b64_tr_b16 v[156:157], v5 offset:0\n"
    "s_mov_b32 m0, s74\n"
    "v_mfma_f32_16x16x32_bf16 a[60:63], v[136:139], a[136:139], a[60:63]\n"
    "ds_read_b64_tr_b16 v[158:159], v5 offset:512\n"
    "s_waitcnt lgkmcnt(2)\n"
    "buffer_load_dwordx4 v1, s[44:47], s65 offen offset:0 lds\n"
    "L_st1_%=:\n"
    "s_cbranch_execz L_st5_%=\n"
    "v_mfma_f32_16x16x32_bf16 a[60:63], v[120:123], a[164:167], a[60:63]\n"
    "ds_read_b64_tr_b16 v[160:161], v6 offset:0\n"
    "v_mfma_f32_16x16x32_bf16 a[60:63], v[104:107], a[160:163], a[60:63]\n"
    "ds_read_b64_tr_b16 v[162:163], v6 offset:512\n"
    "v_mfma_f32_16x16x32_bf16 a[76:79], v[104:107], a[164:167], a[76:79]\n"
    "ds_read_b64_tr_b16 v[164:165], v7 offset:0\n"
    "v_mfma_f32_16x16x32_bf16 a[76:79], v[120:123], a[136:139], a[76:79]\n"
    "ds_read_b64_tr_b16 v[166:167], v7 offset:512\n"
    "buffer_load_dwordx4 v2, s[44:47], s65 offen offset:1024 lds\n"
    "v_mfma_f32_16x16x32_bf16 a[76:79], v[136:139], a[152:155], a[76:79]\n"
    "ds_read_b64_tr_b16 v[168:169], v8 offset:0\n"
    "v_mfma_f32_16x16x32_bf16 a[92:95], v[136:139], a[168:171], a[92:95]\n"
    "ds_read_b64_tr_b16 v[170:171], v8 offset:512\n"
    "v_mfma_f32_16x16x32_bf16 a[92:95], v[120:123], a[152:155], a[92:95]\n"
    "v_mfma_f32_16x16x32_bf16 a[92:95], v[104:107], a[136:139], a[92:95]\n"
    "s_waitcnt vmcnt(18)\n"
    "ds_read_b64_tr_b16 v[172:173], v5 offset:4096\n"
    "buffer_load_dwordx4 v3, s[44:47], s65 offen offset:2048 lds\n"
    "v_mfma_f32_16x16x32_bf16 a[108:111], v[104:107], a[152:155], a[108:111]\n"
    "ds_read_b64_tr_b16 v[174:175], v5 offset:4608\n"
    "v_mfma_f32_16x16x32_bf16 a[108:111], v[120:123], a[168:171], a[108:111]\n"
    "ds_read_b64_tr_b16 v[176:177], v6 offset:4096\n"
    "v_mfma_f32_16x16x32_bf16 a[108:111], v[136:139], a[172:175], a[108:111]\n"
    "ds_read_b64_tr_b16 v[178:179], v6 offset:4608\n"
    "v_mfma_f32_16x16x32_bf16 a[124:127], v[136:139], a[176:179], a[124:127]\n"
    "ds_read_b64_tr_b16 v[180:181], v7 offset:4096\n"
    "buffer_load_dwordx4 v4, s[44:47], s65 offen offset:3072 lds\n"
    "v_mfma_f32_16x16x32_bf16 a[124:127], v[120:123], a[172:175], a[124:127]\n"
    "ds_read_b64_tr_b16 v[182:183], v7 offset:4608\n"
    "v_mfma_f32_16x16x32_bf16 a[124:127], v[104:107], a[168:171], a[124:127]\n"
    "ds_read_b64_tr_b16 v[184:185], v8 offset:4096\n"
    "v_mfma_f32_16x16x32_bf16 a[0:3], v[140:143], a[160:163], a[0:3]\n"
    "ds_read_b64_tr_b16 v[186:187], v8 offset:4608\n"
    "s_mov_b32 m0, s68\n"
    "s_waitcnt lgkmcnt(6)\n"
    "v_mfma_f32_16x16x32_bf16 a[0:3], v[156:159], a[164:167], a[0:3]\n"
    "buffer_load_dwordx4 v1, s[44:47], s66 offen offset:0 lds\n"
    "v_mfma_f32_16x16x32_bf16 a[0:3], v[172:175], a[136:139], a[0:3]\n"
    "v_mfma_f32_16x16x32_bf16 a[16:19], v[172:175], a[152:155], a[16:19]\n"
    "v_mfma_f32_16x16x32_bf16 a[16:19], v[156:159], a[136:139], a[16:19]\n"
    "v_mfma_f32_16x16x32_bf16 a[16:19], v[140:143], a[164:167], a[16:19]\n"
    "buffer_load_dwordx4 v2, s[44:47], s66 offen offset:1024 lds\n"
    "v_mfma_f32_16x16x32_bf16 a[32:35], v[140:143], a[136:139], a[32:35]\n"
    "v_mfma_f32_16x16x32_bf16 a[32:35], v[156:159], a[152:155], a[32:35]\n"
    "v_mfma_f32_16x16x32_bf16 a[32:35], v[172:175], a[168:171], a[32:35]\n"
    "v_mfma_f32_16x16x32_bf16 a[48:51], v[172:175], a[172:175], a[48:51]\n"
    "buffer_load_dwordx4 v3, s[44:47], s66 offen offset:2048 lds\n"
    "v_mfma_f32_16x16x32_bf16 a[48:51], v[156:159], a[168:171], a[48:51]\n"
    "v_mfma_f32_16x16x32_bf16 a[48:51], v[140:143], a[152:155], a[48:51]\n"
    "v_mfma_f32_16x16x32_bf16 a[64:67], v[140:143], a[168:171], a[64:67]\n"
    "v_mfma_f32_16x16x32_bf16 a[64:67], v[156:159], a[172:175], a[64:67]\n"
    "buffer_load_dwordx4 v4, s[44:47], s66 offen offset:3072 lds\n"
    "v_mfma_f32_16x16x32_bf16 a[64:67], v[172:175], a[176:179], a[64:67]\n"
    "v_mfma_f32_16x16x32_bf16 a[80:83], v[172:175], a[180:183], a[80:83]\n"
    "v_mfma_f32_16x16x32_bf16 a[80:83], v[156:159], a[176:179], a[80:83]\n"
    "v_mfma_f32_16x16x32_bf16 a[80:83], v[140:143], a[172:175], a[80:83]\n"
    "v_mfma_f32_16x16x32_bf16 a[96:99], v[140:143], a[176:179], a[96:99]\n"
    "v_mfma_f32_16x16x32_bf16 a[96:99], v[156:159], a[180:183], a[96:99]\n"
    "v_mfma_f32_16x16x32_bf16 a[96:99], v[172:175], a[184:187], a[96:99]\n"
    "v_add_u32 v22, 262144, v22\n"
    "v_mfma_f32_16x16x32_bf16 a[112:115], v[172:175], a[188:191], a[112:115]\n"
    "buffer_load_dword v23, v22, s[44:47], s52 offen\n"
    "v_mfma_f32_16x16x32_bf16 a[112:115], v[156:159], a[184:187], a[112:115]\n"
    "v_mfma_f32_16x16x32_bf16 a[112:115], v[140:143], a[180:183], a[112:115]\n"
    "v_mfma_f32_16x16x32_bf16 a[4:7], v[144:147], a[160:163], a[4:7]\n"
    "v_mfma_f32_16x16x32_bf16 a[4:7], v[160:163], a[164:167], a[4:7]\n"
    "s_waitcnt lgkmcnt(0)\n"
    "v_mfma_f32_16x16x32_bf16 a[4:7], v[176:179], a[136:139], a[4:7]\n"
    "v_mfma_f32_16x16x32_bf16 a[20:23], v[176:179], a[152:155], a[20:23]\n"
    "v_mfma_f32_16x16x32_bf16 a[20:23], v[160:163], a[136:139], a[20:23]\n"
    "v_mfma_f32_16x16x32_bf16 a[20:23], v[144:147], a[164:167], a[20:23]\n"
    "buffer_load_dword v23, v22, s[44:47], s59 offen\n"
    "v_mfma_f32_16x16x32_bf16 a[36:39], v[144:147], a[136:139], a[36:39]\n"
    "v_mfma_f32_16x16x32_bf16 a[36:39], v[160:163], a[152:155], a[36:39]\n"
    "v_mfma_f32_16x16x32_bf16 a[36:39], v[176:179], a[168:171], a[36:39]\n"
    "v_mfma_f32_16x16x32_bf16 a[52:55], v[176:179], a[172:175], a[52:55]\n"
    "v_mfma_f32_16x16x32_bf16 a[52:55], v[160:163], a[168:171], a[52:55]\n"
    "v_mfma_f32_16x16x32_bf16 a[52:55], v[144:147], a[152:155], a[52:55]\n"
    "v_mfma_f32_16x16x32_bf16 a[68:71], v[144:147], a[168:171], a[68:71]\n"
    "v_mfma_f32_16x16x32_bf16 a[68:71], v[160:163], a[172:175], a[68:71]\n"
    "v_mfma_f32_16x16x32_bf16 a[68:71], v[176:179], a[176:179], a[68:71]\n"
    "v_mfma_f32_16x16x32_bf16 a[84:87], v[176:179], a[180:183], a[84:87]\n"
    "v_mfma_f32_16x16x32_bf16 a[84:87], v[160:163], a[176:179], a[84:87]\n"
    "v_mfma_f32_16x16x32_bf16 a[84:87], v[144:147], a[172:175], a[84:87]\n"
    "buffer_load_dword v23, v40, s[40:43], 0 offen offset:256\n"
    "v_mfma_f32_16x16x32_bf16 a[100:103], v[144:147], a[176:179], a[100:103]\n"
    "v_mfma_f32_16x16x32_bf16 a[100:103], v[160:163], a[180:183], a[100:103]\n"
    "v_mfma_f32_16x16x32_bf16 a[100:103], v[176:179], a[184:187], a[100:103]\n"
    "v_mfma_f32_16x16x32_bf16 a[116:119], v[176:179], a[188:191], a[116:119]\n"
    "v_mfma_f32_16x16x32_bf16 a[116:119], v[160:163], a[184:187], a[116:119]\n"
    "v_mfma_f32_16x16x32_bf16 a[116:119], v[144:147], a[180:183], a[116:119]\n"
    "v_mfma_f32_16x16x32_bf16 a[8:11], v[148:151], a[160:163], a[8:11]\n"
    "v_mfma_f32_16x16x32_bf16 a[8:11], v[164:167], a[164:167], a[8:11]\n"
    "v_mfma_f32_16x16x32_bf16 a[8:11], v[180:183], a[136:139], a[8:11]\n"
    "v_mfma_f32_16x16x32_bf16 a[24:27], v[180:183], a[152:155], a[24:27]\n"
    "v_mfma_f32_16x16x32_bf16 a[24:27], v[164:167], a[136:139], a[24:27]\n"
    "v_mfma_f32_16x16x32_bf16 a[24:27], v[148:151], a[164:167], a[24:27]\n"
    "v_mfma_f32_16x16x32_bf16 a[40:43], v[148:151], a[136:139], a[40:43]\n"
    "v_mfma_f32_16x16x32_bf16 a[40:43], v[164:167], a[152:155], a[40:43]\n"
    "v_mfma_f32_16x16x32_bf16 a[40:43], v[180:183], a[168:171], a[40:43]\n"
    "v_mfma_f32_16x16x32_bf16 a[56:59], v[180:183], a[172:175], a[56:59]\n"
    "v_mfma_f32_16x16x32_bf16 a[56:59], v[164:167], a[168:171], a[56:59]\n"
    "v_mfma_f32_16x16x32_bf16 a[56:59], v[148:151], a[152:155], a[56:59]\n"
    "v_mfma_f32_16x16x32_bf16 a[72:75], v[148:151], a[168:171], a[72:75]\n"
    "v_mfma_f32_16x16x32_bf16 a[72:75], v[164:167], a[172:175], a[72:75]\n"
    "v_mfma_f32_16x16x32_bf16 a[72:75], v[180:183], a[176:179], a[72:75]\n"
    "v_mfma_f32_16x16x32_bf16 a[88:91], v[180:183], a[180:183], a[88:91]\n"
    "v_mfma_f32_16x16x32_bf16 a[88:91], v[164:167], a[176:179], a[88:91]\n"
    "v_mfma_f32_16x16x32_bf16 a[88:91], v[148:151], a[172:175], a[88:91]\n"
    "v_mfma_f32_16x16x32_bf16 a[104:107], v[148:151], a[176:179], a[104:107]\n"
    "v_mfma_f32_16x16x32_bf16 a[104:107], v[164:167], a[180:183], a[104:107]\n"
    "v_mfma_f32_16x16x32_bf16 a[104:107], v[180:183], a[184:187], a[104:107]\n"
    "v_mfma_f32_16x16x32_bf16 a[120:123], v[180:183], a[188:191], a[120:123]\n"
    "v_mfma_f32_16x16x32_bf16 a[120:123], v[164:167], a[184:187], a[120:123]\n"
    "v_mfma_f32_16x16x32_bf16 a[120:123], v[148:151], a[180:183], a[120:123]\n"
    "s_waitcnt vmcnt(23)\n"
    "ds_read_b64_tr_b16 v[188:189], v5 offset:8192\n"
    "v_mfma_f32_16x16x32_bf16 a[12:15], v[152:155], a[160:163], a[12:15]\n"
    "ds_read_b64_tr_b16 v[190:191], v5 offset:8704\n"
    "v_mfma_f32_16x16x32_bf16 a[12:15], v[168:171], a[164:167], a[12:15]\n"
    "ds_read_b64_tr_b16 v[192:193], v6 offset:8192\n"
    "v_mfma_f32_16x16x32_bf16 a[12:15], v[184:187], a[136:139], a[12:15]\n"
    "ds_read_b64_tr_b16 v[194:195], v6 offset:8704\n"
    "v_mfma_f32_16x16x32_bf16 a[28:31], v[184:187], a[152:155], a[28:31]\n"
    "ds_read_b64_tr_b16 v[196:197], v7 offset:8192\n"
    "v_mfma_f32_16x16x32_bf16 a[28:31], v[168:171], a[136:139], a[28:31]\n"
    "ds_read_b64_tr_b16 v[198:199], v7 offset:8704\n"
    "v_mfma_f32_16x16x32_bf16 a[28:31], v[152:155], a[164:167], a[28:31]\n"
    "ds_read_b64_tr_b16 v[200:201], v8 offset:8192\n"
    "v_mfma_f32_16x16x32_bf16 a[44:47], v[152:155], a[136:139], a[44:47]\n"
    "ds_read_b64_tr_b16 v[202:203], v8 offset:8704\n"
    "v_mfma_f32_16x16x32_bf16 a[44:47], v[168:171], a[152:155], a[44:47]\n"
    "v_mfma_f32_16x16x32_bf16 a[44:47], v[184:187], a[168:171], a[44:47]\n"
    "s_waitcnt vmcnt(19)\n"
    "ds_read_b64_tr_b16 v[204:205], v5 offset:12288\n"
    "v_mfma_f32_16x16x32_bf16 a[60:63], v[184:187], a[172:175], a[60:63]\n"
    "ds_read_b64_tr_b16 v[206:207], v5 offset:12800\n"
    "v_mfma_f32_16x16x32_bf16 a[60:63], v[168:171], a[168:171], a[60:63]\n"
    "ds_read_b64_tr_b16 v[208:209], v6 offset:12288\n"
    "v_mfma_f32_16x16x32_bf16 a[60:63], v[152:155], a[152:155], a[60:63]\n"
    "ds_read_b64_tr_b16 v[210:211], v6 offset:12800\n"
    "v_mfma_f32_16x16x32_bf16 a[76:79], v[152:155], a[168:171], a[76:79]\n"
    "ds_read_b64_tr_b16 v[212:213], v7 offset:12288\n"
    "v_mfma_f32_16x16x32_bf16 a[76:79], v[168:171], a[172:175], a[76:79]\n"
    "ds_read_b64_tr_b16 v[214:215], v7 offset:12800\n"
    "v_mfma_f32_16x16x32_bf16 a[76:79], v[184:187], a[176:179], a[76:79]\n"
    "ds_read_b64_tr_b16 v[216:217], v8 offset:12288\n"
    "v_mfma_f32_16x16x32_bf16 a[92:95], v[184:187], a[180:183], a[92:95]\n"
    "ds_read_b64_tr_b16 v[218:219], v8 offset:12800\n"
    "v_mfma_f32_16x16x32_bf16 a[92:95], v[168:171], a[176:179], a[92:95]\n"
    "v_mfma_f32_16x16x32_bf16 a[92:95], v[152:155], a[172:175], a[92:95]\n"
    "s_waitcnt vmcnt(15)\n"
    "ds_read_b64_tr_b16 v[220:221], v5 offset:16384\n"
    "v_mfma_f32_16x16x32_bf16 a[108:111], v[152:155], a[176:179], a[108:111]\n"
    "ds_read_b64_tr_b16 v[222:223], v5 offset:16896\n"
    "v_mfma_f32_16x16x32_bf16 a[108:111], v[168:171], a[180:183], a[108:111]\n"
    "ds_read_b64_tr_b16 v[224:225], v6 offset:16384\n"
    "v_mfma_f32_16x16x32_bf16 a[108:111], v[184:187], a[184:187], a[108:111]\n"
    "ds_read_b64_tr_b16 v[226:227], v6 offset:16896\n"
    "v_mfma_f32_16x16x32_bf16 a[124:127], v[184:187], a[188:191], a[124:127]\n"
    "ds_read_b64_tr_b16 v[228:229], v7 offset:16384\n"
    "v_mfma_f32_16x16x32_bf16 a[124:127], v[168:171], a[184:187], a[124:127]\n"
    "ds_read_b64_tr_b16 v[230:231], v7 offset:16896\n"
    "v_mfma_f32_16x16x32_bf16 a[124:127], v[152:155], a[180:183], a[124:127]\n"
    "ds_read_b64_tr_b16 v[232:233], v8 offset:16384\n"
    "s_waitcnt lgkmcnt(6)\n"
    "v_mfma_f32_16x16x32_bf16 a[0:3], v[188:191], a[152:155], a[0:3]\n"
    "ds_read_b64_tr_b16 v[234:235], v8 offset:16896\n"
    "v_mfma_f32_16x16x32_bf16 a[0:3], v[204:207], a[168:171], a[0:3]\n"
    "s_waitcnt lgkmcnt(5)\n"
    "v_mfma_f32_16x16x32_bf16 a[0:3], v[220:223], a[172:175], a[0:3]\n"
    "v_mfma_f32_16x16x32_bf16 a[16:19], v[220:223], a[176:179], a[16:19]\n"
    "v_mfma_f32_16x16x32_bf16 a[16:19], v[204:207], a[172:175], a[16:19]\n"
    "v_mfma_f32_16x16x32_bf16 a[16:19], v[188:191], a[168:171], a[16:19]\n"
    "v_mfma_f32_16x16x32_bf16 a[32:35], v[188:191], a[172:175], a[32:35]\n"
    "v_mfma_f32_16x16x32_bf16 a[32:35], v[204:207], a[176:179], a[32:35]\n"
    "v_mfma_f32_16x16x32_bf16 a[32:35], v[220:223], a[180:183], a[32:35]\n"
    "v_mfma_f32_16x16x32_bf16 a[48:51], v[220:223], a[184:187], a[48:51]\n"
    "v_mfma_f32_16x16x32_bf16 a[48:51], v[204:207], a[180:183], a[48:51]\n"
    "v_mfma_f32_16x16x32_bf16 a[48:51], v[188:191], a[176:179], a[48:51]\n"
    "v_mfma_f32_16x16x32_bf16 a[64:67], v[188:191], a[180:183], a[64:67]\n"
    "v_mfma_f32_16x16x32_bf16 a[64:67], v[204:207], a[184:187], a[64:67]\n"
    "v_mfma_f32_16x16x32_bf16 a[64:67], v[220:223], a[188:191], a[64:67]\n"
    "v_mfma_f32_16x16x32_bf16 a[80:83], v[220:223], a[192:195], a[80:83]\n"
    "v_mfma_f32_16x16x32_bf16 a[80:83], v[204:207], a[188:191], a[80:83]\n"
    "v_mfma_f32_16x16x32_bf16 a[80:83], v[188:191], a[184:187], a[80:83]\n"
    "v_mfma_f32_16x16x32_bf16 a[96:99], v[188:191], a[188:191], a[96:99]\n"
    "v_mfma_f32_16x16x32_bf16 a[96:99], v[204:207], a[192:195], a[96:99]\n"
    "v_mfma_f32_16x16x32_bf16 a[96:99], v[220:223], a[196:199], a[96:99]\n"
    "v_mfma_f32_16x16x32_bf16 a[112:115], v[220:223], a[200:203], a[112:115]\n"
    "v_mfma_f32_16x16x32_bf16 a[112:115], v[204:207], a[196:199], a[112:115]\n"
    "v_mfma_f32_16x16x32_bf16 a[112:115], v[188:191], a[192:195], a[112:115]\n"
    "v_mfma_f32_16x16x32_bf16 a[4:7], v[192:195], a[152:155], a[4:7]\n"
    "v_mfma_f32_16x16x32_bf16 a[4:7], v[208:211], a[168:171], a[4:7]\n"
    "s_waitcnt lgkmcnt(0)\n"
    "v_mfma_f32_16x16x32_bf16 a[4:7], v[224:227], a[172:175], a[4:7]\n"
    "v_mfma_f32_16x16x32_bf16 a[20:23], v[224:227], a[176:179], a[20:23]\n"
    "v_mfma_f32_16x16x32_bf16 a[20:23], v[208:211], a[172:175], a[20:23]\n"
    "v_mfma_f32_16x16x32_bf16 a[20:23], v[192:195], a[168:171], a[20:23]\n"
    "v_mfma_f32_16x16x32_bf16 a[36:39], v[192:195], a[172:175], a[36:39]\n"
    "v_mfma_f32_16x16x32_bf16 a[36:39], v[208:211], a[176:179], a[36:39]\n"
    "v_mfma_f32_16x16x32_bf16 a[36:39], v[224:227], a[180:183], a[36:39]\n"
    "v_mfma_f32_16x16x32_bf16 a[52:55], v[224:227], a[184:187], a[52:55]\n"
    "v_mfma_f32_16x16x32_bf16 a[52:55], v[208:211], a[180:183], a[52:55]\n"
    "v_mfma_f32_16x16x32_bf16 a[52:55], v[192:195], a[176:179], a[52:55]\n"
    "v_mfma_f32_16x16x32_bf16 a[68:71], v[192:195], a[180:183], a[68:71]\n"
    "v_mfma_f32_16x16x32_bf16 a[68:71], v[208:211], a[184:187], a[68:71]\n"
    "v_mfma_f32_16x16x32_bf16 a[68:71], v[224:227], a[188:191], a[68:71]\n"
    "v_mfma_f32_16x16x32_bf16 a[84:87], v[224:227], a[192:195], a[84:87]\n"
    "v_mfma_f32_16x16x32_bf16 a[84:87], v[208:211], a[188:191], a[84:87]\n"
    "v_mfma_f32_16x16x32_bf16 a[84:87], v[192:195], a[184:187], a[84:87]\n"
    "v_mfma_f32_16x16x32_bf16 a[100:103], v[192:195], a[188:191], a[100:103]\n"
    "v_mfma_f32_16x16x32_bf16 a[100:103], v[208:211], a[192:195], a[100:103]\n"
    "v_mfma_f32_16x16x32_bf16 a[100:103], v[224:227], a[196:199], a[100:103]\n"
    "v_mfma_f32_16x16x32_bf16 a[116:119], v[224:227], a[200:203], a[116:119]\n"
    "v_mfma_f32_16x16x32_bf16 a[116:119], v[208:211], a[196:199], a[116:119]\n"
    "v_mfma_f32_16x16x32_bf16 a[116:119], v[192:195], a[192:195], a[116:119]\n"
    "v_mfma_f32_16x16x32_bf16 a[8:11], v[196:199], a[152:155], a[8:11]\n"
    "v_mfma_f32_16x16x32_bf16 a[8:11], v[212:215], a[168:171], a[8:11]\n"
    "v_mfma_f32_16x16x32_bf16 a[8:11], v[228:231], a[172:175], a[8:11]\n"
    "v_mfma_f32_16x16x32_bf16 a[24:27], v[228:231], a[176:179], a[24:27]\n"
    "v_mfma_f32_16x16x32_bf16 a[24:27], v[212:215], a[172:175], a[24:27]\n"
    "v_mfma_f32_16x16x32_bf16 a[24:27], v[196:199], a[168:171], a[24:27]\n"
    "v_mfma_f32_16x16x32_bf16 a[40:43], v[196:199], a[172:175], a[40:43]\n"
    "v_mfma_f32_16x16x32_bf16 a[40:43], v[212:215], a[176:179], a[40:43]\n"
    "v_mfma_f32_16x16x32_bf16 a[40:43], v[228:231], a[180:183], a[40:43]\n"
    "v_mfma_f32_16x16x32_bf16 a[56:59], v[228:231], a[184:187], a[56:59]\n"
    "v_mfma_f32_16x16x32_bf16 a[56:59], v[212:215], a[180:183], a[56:59]\n"
    "v_mfma_f32_16x16x32_bf16 a[56:59], v[196:199], a[176:179], a[56:59]\n"
    "v_mfma_f32_16x16x32_bf16 a[72:75], v[196:199], a[180:183], a[72:75]\n"
    "v_mfma_f32_16x16x32_bf16 a[72:75], v[212:215], a[184:187], a[72:75]\n"
    "v_mfma_f32_16x16x32_bf16 a[72:75], v[228:231], a[188:191], a[72:75]\n"
    "v_mfma_f32_16x16x32_bf16 a[88:91], v[228:231], a[192:195], a[88:91]\n"
    "s_mov_b32 m0, s69\n"
    "v_mfma_f32_16x16x32_bf16 a[88:91], v[212:215], a[188:191], a[88:91]\n"
    "v_add_u32 v3, 0x40000, v3\n"
    "v_add_u32 v4, 0x40000, v4\n"
    "v_mfma_f32_16x16x32_bf16 a[88:91], v[196:199], a[184:187], a[88:91]\n"
    "v_add_u32 v1, 0x40000, v1\n"
    "v_add_u32 v2, 0x40000, v2\n"
    "v_mfma_f32_16x16x32_bf16 a[104:107], v[196:199], a[188:191], a[104:107]\n"
    "buffer_load_dwordx4 v1, s[44:47], s52 offen offset:0 lds\n"
    "v_mfma_f32_16x16x32_bf16 a[104:107], v[212:215], a[192:195], a[104:107]\n"
    "v_mfma_f32_16x16x32_bf16 a[104:107], v[228:231], a[196:199], a[104:107]\n"
    "v_mfma_f32_16x16x32_bf16 a[120:123], v[228:231], a[200:203], a[120:123]\n"
    "v_mfma_f32_16x16x32_bf16 a[120:123], v[212:215], a[196:199], a[120:123]\n"
    "buffer_load_dwordx4 v2, s[44:47], s52 offen offset:1024 lds\n"
    "v_mfma_f32_16x16x32_bf16 a[120:123], v[196:199], a[192:195], a[120:123]\n"
    "s_waitcnt vmcnt(13)\n"
    "ds_read_b64_tr_b16 v[236:237], v5 offset:20480\n"
    "v_mfma_f32_16x16x32_bf16 a[12:15], v[200:203], a[152:155], a[12:15]\n"
    "ds_read_b64_tr_b16 v[238:239], v5 offset:20992\n"
    "v_mfma_f32_16x16x32_bf16 a[12:15], v[216:219], a[168:171], a[12:15]\n"
    "ds_read_b64_tr_b16 v[240:241], v6 offset:20480\n"
    "v_mfma_f32_16x16x32_bf16 a[12:15], v[232:235], a[172:175], a[12:15]\n"
    "ds_read_b64_tr_b16 v[242:243], v6 offset:20992\n"
    "buffer_load_dwordx4 v3, s[44:47], s52 offen offset:2048 lds\n"
    "v_mfma_f32_16x16x32_bf16 a[28:31], v[232:235], a[176:179], a[28:31]\n"
    "ds_read_b64_tr_b16 v[244:245], v7 offset:20480\n"
    "v_mfma_f32_16x16x32_bf16 a[28:31], v[216:219], a[172:175], a[28:31]\n"
    "ds_read_b64_tr_b16 v[246:247], v7 offset:20992\n"
    "v_mfma_f32_16x16x32_bf16 a[28:31], v[200:203], a[168:171], a[28:31]\n"
    "ds_read_b64_tr_b16 v[248:249], v8 offset:20480\n"
    "v_mfma_f32_16x16x32_bf16 a[44:47], v[200:203], a[172:175], a[44:47]\n"
    "ds_read_b64_tr_b16 v[250:251], v8 offset:20992\n"
    "buffer_load_dwordx4 v4, s[44:47], s52 offen offset:3072 lds\n"
    "v_mfma_f32_16x16x32_bf16 a[44:47], v[216:219], a[176:179], a[44:47]\n"
    "v_mfma_f32_16x16x32_bf16 a[44:47], v[232:235], a[180:183], a[44:47]\n"
    "s_waitcnt vmcnt(11)\n"
    "ds_read_b64_tr_b16 v[252:253], v5 offset:24576\n"
    "v_mfma_f32_16x16x32_bf16 a[60:63], v[232:235], a[184:187], a[60:63]\n"
    "ds_read_b64_tr_b16 v[254:255], v5 offset:25088\n"
    "s_add_u32 m0, s75, 28416\n"
    "v_mfma_f32_16x16x32_bf16 a[60:63], v[216:219], a[180:183], a[60:63]\n"
    "ds_read_b64_tr_b16 v[44:45], v6 offset:24576\n"
    "buffer_load_dwordx4 v12, s[40:43], 0 offen offset:256 lds\n"
    "v_mfma_f32_16x16x32_bf16 a[60:63], v[200:203], a[176:179], a[60:63]\n"
    "ds_read_b64_tr_b16 v[46:47], v6 offset:25088\n"
    "v_mfma_f32_16x16x32_bf16 a[76:79], v[200:203], a[180:183], a[76:79]\n"
    "ds_read_b64_tr_b16 v[48:49], v7 offset:24576\n"
    "v_mfma_f32_16x16x32_bf16 a[76:79], v[216:219], a[184:187], a[76:79]\n"
    "ds_read_b64_tr_b16 v[50:51], v7 offset:25088\n"
    "s_add_u32 m0, s75, 29440\n"
    "v_mfma_f32_16x16x32_bf16 a[76:79], v[232:235], a[188:191], a[76:79]\n"
    "ds_read_b64_tr_b16 v[52:53], v8 offset:24576\n"
    "buffer_load_dwordx4 v13, s[40:43], 0 offen offset:256 lds\n"
    "v_mfma_f32_16x16x32_bf16 a[92:95], v[232:235], a[192:195], a[92:95]\n"
    "ds_read_b64_tr_b16 v[54:55], v8 offset:25088\n"
    "v_mfma_f32_16x16x32_bf16 a[92:95], v[216:219], a[188:191], a[92:95]\n"
    "v_mfma_f32_16x16x32_bf16 a[92:95], v[200:203], a[184:187], a[92:95]\n"
    "s_waitcnt vmcnt(9)\n"
    "ds_read_b64_tr_b16 v[56:57], v5 offset:0\n"
    "s_add_u32 m0, s75, 30464\n"
    "v_mfma_f32_16x16x32_bf16 a[108:111], v[200:203], a[188:191], a[108:111]\n"
    "ds_read_b64_tr_b16 v[58:59], v5 offset:512\n"
    "buffer_load_dwordx4 v14, s[40:43], 0 offen offset:256 lds\n"
    "v_mfma_f32_16x16x32_bf16 a[108:111], v[216:219], a[192:195], a[108:111]\n"
    "ds_read_b64_tr_b16 v[60:61], v6 offset:0\n"
    "v_mfma_f32_16x16x32_bf16 a[108:111], v[232:235], a[196:199], a[108:111]\n"
    "ds_read_b64_tr_b16 v[62:63], v6 offset:512\n"
    "v_mfma_f32_16x16x32_bf16 a[124:127], v[232:235], a[200:203], a[124:127]\n"
    "ds_read_b64_tr_b16 v[64:65], v7 offset:0\n"
    "s_add_u32 m0, s75, 31488\n"
    "v_mfma_f32_16x16x32_bf16 a[124:127], v[216:219], a[196:199], a[124:127]\n"
    "ds_read_b64_tr_b16 v[66:67], v7 offset:512\n"
    "buffer_load_dwordx4 v15, s[40:43], 0 offen offset:256 lds\n"
    "v_mfma_f32_16x16x32_bf16 a[124:127], v[200:203], a[192:195], a[124:127]\n"
    "ds_read_b64_tr_b16 v[68:69], v8 offset:0\n"
    "s_waitcnt lgkmcnt(6)\n"
    "v_mfma_f32_16x16x32_bf16 a[0:3], v[236:239], a[176:179], a[0:3]\n"
    "ds_read_b64_tr_b16 v[70:71], v8 offset:512\n"
    "v_mfma_f32_16x16x32_bf16 a[0:3], v[252:255], a[180:183], a[0:3]\n"
    "s_mov_b32 m0, s70\n"
    "s_waitcnt lgkmcnt(5)\n"
    "v_mfma_f32_16x16x32_bf16 a[0:3], v[56:59], a[184:187], a[0:3]\n"
    "buffer_load_dwordx4 v1, s[44:47], s53 offen offset:0 lds\n"
    "v_mfma_f32_16x16x32_bf16 a[16:19], v[56:59], a[188:191], a[16:19]\n"
    "v_mfma_f32_16x16x32_bf16 a[16:19], v[252:255], a[184:187], a[16:19]\n"
    "v_mfma_f32_16x16x32_bf16 a[16:19], v[236:239], a[180:183], a[16:19]\n"
    "v_mfma_f32_16x16x32_bf16 a[32:35], v[236:239], a[184:187], a[32:35]\n"
    "buffer_load_dwordx4 v2, s[44:47], s53 offen offset:1024 lds\n"
    "v_mfma_f32_16x16x32_bf16 a[32:35], v[252:255], a[188:191], a[32:35]\n"
    "v_mfma_f32_16x16x32_bf16 a[32:35], v[56:59], a[192:195], a[32:35]\n"
    "v_mfma_f32_16x16x32_bf16 a[48:51], v[56:59], a[196:199], a[48:51]\n"
    "v_mfma_f32_16x16x32_bf16 a[48:51], v[252:255], a[192:195], a[48:51]\n"
    "buffer_load_dwordx4 v3, s[44:47], s53 offen offset:2048 lds\n"
    "v_mfma_f32_16x16x32_bf16 a[48:51], v[236:239], a[188:191], a[48:51]\n"
    "v_mfma_f32_16x16x32_bf16 a[64:67], v[236:239], a[192:195], a[64:67]\n"
    "v_mfma_f32_16x16x32_bf16 a[64:67], v[252:255], a[196:199], a[64:67]\n"
    "v_mfma_f32_16x16x32_bf16 a[64:67], v[56:59], a[200:203], a[64:67]\n"
    "buffer_load_dwordx4 v4, s[44:47], s53 offen offset:3072 lds\n"
    "v_mfma_f32_16x16x32_bf16 a[80:83], v[56:59], a[204:207], a[80:83]\n"
    "v_mfma_f32_16x16x32_bf16 a[80:83], v[252:255], a[200:203], a[80:83]\n"
    "v_mfma_f32_16x16x32_bf16 a[80:83], v[236:239], a[196:199], a[80:83]\n"
    "s_add_u32 m0, s75, 32512\n"
    "v_mfma_f32_16x16x32_bf16 a[96:99], v[236:239], a[200:203], a[96:99]\n"
    "buffer_load_dwordx4 v16, s[40:43], 0 offen offset:256 lds\n"
    "v_mfma_f32_16x16x32_bf16 a[96:99], v[252:255], a[204:207], a[96:99]\n"
    "v_mfma_f32_16x16x32_bf16 a[96:99], v[56:59], a[208:211], a[96:99]\n"
    "v_mfma_f32_16x16x32_bf16 a[112:115], v[56:59], a[212:215], a[112:115]\n"
    "s_add_u32 m0, s75, 33536\n"
    "v_mfma_f32_16x16x32_bf16 a[112:115], v[252:255], a[208:211], a[112:115]\n"
    "buffer_load_dwordx4 v17, s[40:43], 0 offen offset:256 lds\n"
    "v_mfma_f32_16x16x32_bf16 a[112:115], v[236:239], a[204:207], a[112:115]\n"
    "v_mfma_f32_16x16x32_bf16 a[4:7], v[240:243], a[176:179], a[4:7]\n"
    "v_mfma_f32_16x16x32_bf16 a[4:7], v[44:47], a[180:183], a[4:7]\n"
    "s_add_u32 m0, s75, 34560\n"
    "s_waitcnt lgkmcnt(0)\n"
    "v_mfma_f32_16x16x32_bf16 a[4:7], v[60:63], a[184:187], a[4:7]\n"
    "buffer_load_dwordx4 v18, s[40:43], 0 offen offset:256 lds\n"
    "v_mfma_f32_16x16x32_bf16 a[20:23], v[60:63], a[188:191], a[20:23]\n"
    "v_mfma_f32_16x16x32_bf16 a[20:23], v[44:47], a[184:187], a[20:23]\n"
    "v_mfma_f32_16x16x32_bf16 a[20:23], v[240:243], a[180:183], a[20:23]\n"
    "s_add_u32 m0, s75, 35584\n"
    "v_mfma_f32_16x16x32_bf16 a[36:39], v[240:243], a[184:187], a[36:39]\n"
    "buffer_load_dwordx4 v19, s[40:43], 0 offen offset:256 lds\n"
    "v_mfma_f32_16x16x32_bf16 a[36:39], v[44:47], a[188:191], a[36:39]\n"
    "v_mfma_f32_16x16x32_bf16 a[36:39], v[60:63], a[192:195], a[36:39]\n"
    "v_mfma_f32_16x16x32_bf16 a[52:55], v[60:63], a[196:199], a[52:55]\n"
    "s_mov_b32 m0, s71\n"
    "v_mfma_f32_16x16x32_bf16 a[52:55], v[44:47], a[192:195], a[52:55]\n"
    "buffer_load_dwordx4 v1, s[44:47], s54 offen offset:0 lds\n"
    "v_mfma_f32_16x16x32_bf16 a[52:55], v[240:243], a[188:191], a[52:55]\n"
    "v_mfma_f32_16x16x32_bf16 a[68:71], v[240:243], a[192:195], a[68:71]\n"
    "v_mfma_f32_16x16x32_bf16 a[68:71], v[44:47], a[196:199], a[68:71]\n"
    "v_mfma_f32_16x16x32_bf16 a[68:71], v[60:63], a[200:203], a[68:71]\n"
    "buffer_load_dwordx4 v2, s[44:47], s54 offen offset:1024 lds\n"
    "v_mfma_f32_16x16x32_bf16 a[84:87], v[60:63], a[204:207], a[84:87]\n"
    "v_mfma_f32_16x16x32_bf16 a[84:87], v[44:47], a[200:203], a[84:87]\n"
    "v_mfma_f32_16x16x32_bf16 a[84:87], v[240:243], a[196:199], a[84:87]\n"
    "v_mfma_f32_16x16x32_bf16 a[100:103], v[240:243], a[200:203], a[100:103]\n"
    "buffer_load_dwordx4 v3, s[44:47], s54 offen offset:2048 lds\n"
    "v_mfma_f32_16x16x32_bf16 a[100:103], v[44:47], a[204:207], a[100:103]\n"
    "v_mfma_f32_16x16x32_bf16 a[100:103], v[60:63], a[208:211], a[100:103]\n"
    "v_mfma_f32_16x16x32_bf16 a[116:119], v[60:63], a[212:215], a[116:119]\n"
    "v_mfma_f32_16x16x32_bf16 a[116:119], v[44:47], a[208:211], a[116:119]\n"
    "buffer_load_dwordx4 v4, s[44:47], s54 offen offset:3072 lds\n"
    "v_mfma_f32_16x16x32_bf16 a[116:119], v[240:243], a[204:207], a[116:119]\n"
    "v_mfma_f32_16x16x32_bf16 a[8:11], v[244:247], a[176:179], a[8:11]\n"
    "v_mfma_f32_16x16x32_bf16 a[8:11], v[48:51], a[180:183], a[8:11]\n"
    "s_add_u32 m0, s75, 36608\n"
    "v_mfma_f32_16x16x32_bf16 a[8:11], v[64:67], a[184:187], a[8:11]\n"
    "buffer_load_dwordx4 v20, s[40:43], 0 offen offset:256 lds\n"
    "v_mfma_f32_16x16x32_bf16 a[24:27], v[64:67], a[188:191], a[24:27]\n"
    "v_mfma_f32_16x16x32_bf16 a[24:27], v[48:51], a[184:187], a[24:27]\n"
    "v_mfma_f32_16x16x32_bf16 a[24:27], v[244:247], a[180:183], a[24:27]\n"
    "s_add_u32 m0, s75, 37632\n"
    "v_mfma_f32_16x16x32_bf16 a[40:43], v[244:247], a[184:187], a[40:43]\n"
    "buffer_load_dwordx4 v21, s[40:43], 0 offen offset:256 lds\n"
    "v_mfma_f32_16x16x32_bf16 a[40:43], v[48:51], a[188:191], a[40:43]\n"
    "v_mfma_f32_16x16x32_bf16 a[40:43], v[64:67], a[192:195], a[40:43]\n"
    "v_mfma_f32_16x16x32_bf16 a[56:59], v[64:67], a[196:199], a[56:59]\n"
    "s_mov_b32 m0, s72\n"
    "v_mfma_f32_16x16x32_bf16 a[56:59], v[48:51], a[192:195], a[56:59]\n"
    "buffer_load_dwordx4 v1, s[44:47], s55 offen offset:0 lds\n"
    "v_mfma_f32_16x16x32_bf16 a[56:59], v[244:247], a[188:191], a[56:59]\n"
    "v_mfma_f32_16x16x32_bf16 a[72:75], v[244:247], a[192:195], a[72:75]\n"
    "v_mfma_f32_16x16x32_bf16 a[72:75], v[48:51], a[196:199], a[72:75]\n"
    "v_mfma_f32_16x16x32_bf16 a[72:75], v[64:67], a[200:203], a[72:75]\n"
    "buffer_load_dwordx4 v2, s[44:47], s55 offen offset:1024 lds\n"
    "v_mfma_f32_16x16x32_bf16 a[88:91], v[64:67], a[204:207], a[88:91]\n"
    "v_mfma_f32_16x16x32_bf16 a[88:91], v[48:51], a[200:203], a[88:91]\n"
    "v_mfma_f32_16x16x32_bf16 a[88:91], v[244:247], a[196:199], a[88:91]\n"
    "v_mfma_f32_16x16x32_bf16 a[104:107], v[244:247], a[200:203], a[104:107]\n"
    "buffer_load_dwordx4 v3, s[44:47], s55 offen offset:2048 lds\n"
    "v_mfma_f32_16x16x32_bf16 a[104:107], v[48:51], a[204:207], a[104:107]\n"
    "v_mfma_f32_16x16x32_bf16 a[104:107], v[64:67], a[208:211], a[104:107]\n"
    "v_mfma_f32_16x16x32_bf16 a[120:123], v[64:67], a[212:215], a[120:123]\n"
    "v_mfma_f32_16x16x32_bf16 a[120:123], v[48:51], a[208:211], a[120:123]\n"
    "buffer_load_dwordx4 v4, s[44:47], s55 offen offset:3072 lds\n"
    "v_mfma_f32_16x16x32_bf16 a[120:123], v[244:247], a[204:207], a[120:123]\n"
    "s_waitcnt vmcnt(22)\n"
    "ds_read_b64_tr_b16 v[72:73], v5 offset:4096\n"
    "v_mfma_f32_16x16x32_bf16 a[12:15], v[248:251], a[176:179], a[12:15]\n"
    "ds_read_b64_tr_b16 v[74:75], v5 offset:4608\n"
    "v_mfma_f32_16x16x32_bf16 a[12:15], v[52:55], a[180:183], a[12:15]\n"
    "ds_read_b64_tr_b16 v[76:77], v6 offset:4096\n"
    "s_mov_b32 m0, s73\n"
    "v_mfma_f32_16x16x32_bf16 a[12:15], v[68:71], a[184:187], a[12:15]\n"
    "ds_read_b64_tr_b16 v[78:79], v6 offset:4608\n"
    "buffer_load_dwordx4 v1, s[44:47], s56 offen offset:0 lds\n"
    "v_mfma_f32_16x16x32_bf16 a[28:31], v[68:71], a[188:191], a[28:31]\n"
    "ds_read_b64_tr_b16 v[80:81], v7 offset:4096\n"
    "v_mfma_f32_16x16x32_bf16 a[28:31], v[52:55], a[184:187], a[28:31]\n"
    "ds_read_b64_tr_b16 v[82:83], v7 offset:4608\n"
    "v_mfma_f32_16x16x32_bf16 a[28:31], v[248:251], a[180:183], a[28:31]\n"
    "ds_read_b64_tr_b16 v[84:85], v8 offset:4096\n"
    "v_mfma_f32_16x16x32_bf16 a[44:47], v[248:251], a[184:187], a[44:47]\n"
    "ds_read_b64_tr_b16 v[86:87], v8 offset:4608\n"
    "buffer_load_dwordx4 v2, s[44:47], s56 offen offset:1024 lds\n"
    "v_mfma_f32_16x16x32_bf16 a[44:47], v[52:55], a[188:191], a[44:47]\n"
    "v_mfma_f32_16x16x32_bf16 a[44:47], v[68:71], a[192:195], a[44:47]\n"
    "s_waitcnt vmcnt(16)\n"
    "ds_read_b64_tr_b16 v[88:89], v5 offset:8192\n"
    "v_mfma_f32_16x16x32_bf16 a[60:63], v[68:71], a[196:199], a[60:63]\n"
    "ds_read_b64_tr_b16 v[90:91], v5 offset:8704\n"
    "v_mfma_f32_16x16x32_bf16 a[60:63], v[52:55], a[192:195], a[60:63]\n"
    "ds_read_b64_tr_b16 v[124:125], v6 offset:8192\n"
    "buffer_load_dwordx4 v3, s[44:47], s56 offen offset:2048 lds\n"
    "v_mfma_f32_16x16x32_bf16 a[60:63], v[248:251], a[188:191], a[60:63]\n"
    "ds_read_b64_tr_b16 v[126:127], v6 offset:8704\n"
    "v_mfma_f32_16x16x32_bf16 a[76:79], v[248:251], a[192:195], a[76:79]\n"
    "ds_read_b64_tr_b16 v[108:109], v7 offset:8192\n"
    "v_mfma_f32_16x16x32_bf16 a[76:79], v[52:55], a[196:199], a[76:79]\n"
    "ds_read_b64_tr_b16 v[110:111], v7 offset:8704\n"
    "v_mfma_f32_16x16x32_bf16 a[76:79], v[68:71], a[200:203], a[76:79]\n"
    "ds_read_b64_tr_b16 v[92:93], v8 offset:8192\n"
    "buffer_load_dwordx4 v4, s[44:47], s56 offen offset:3072 lds\n"
    "v_mfma_f32_16x16x32_bf16 a[92:95], v[68:71], a[204:207], a[92:95]\n"
    "ds_read_b64_tr_b16 v[94:95], v8 offset:8704\n"
    "v_mfma_f32_16x16x32_bf16 a[92:95], v[52:55], a[200:203], a[92:95]\n"
    "ds_read_b128 a[216:219], v9 offset:28672\n"
    "s_mov_b32 m0, s74\n"
    "v_mfma_f32_16x16x32_bf16 a[92:95], v[248:251], a[196:199], a[92:95]\n"
    "s_waitcnt vmcnt(10)\n"
    "ds_read_b64_tr_b16 v[128:129], v5 offset:12288\n"
    "ds_read_b128 a[220:223], v9 offset:29952\n"
    "v_mfma_f32_16x16x32_bf16 a[108:111], v[248:251], a[200:203], a[108:111]\n"
    "ds_read_b64_tr_b16 v[130:131], v5 offset:12800\n"
    "ds_read_b128 a[224:227], v9 offset:31232\n"
    "v_mfma_f32_16x16x32_bf16 a[108:111], v[52:55], a[204:207], a[108:111]\n"
    "ds_read_b64_tr_b16 v[112:113], v6 offset:12288\n"
    "ds_read_b128 a[228:231], v9 offset:32512\n"
    "v_mfma_f32_16x16x32_bf16 a[108:111], v[68:71], a[208:211], a[108:111]\n"
    "ds_read_b64_tr_b16 v[114:115], v6 offset:12800\n"
    "buffer_load_dwordx4 v1, s[44:47], s57 offen offset:0 lds\n"
    "v_mfma_f32_16x16x32_bf16 a[124:127], v[68:71], a[212:215], a[124:127]\n"
    "ds_read_b64_tr_b16 v[96:97], v7 offset:12288\n"
    "v_mfma_f32_16x16x32_bf16 a[124:127], v[52:55], a[208:211], a[124:127]\n"
    "ds_read_b64_tr_b16 v[98:99], v7 offset:12800\n"
    "v_mfma_f32_16x16x32_bf16 a[124:127], v[248:251], a[204:207], a[124:127]\n"
    "ds_read_b64_tr_b16 v[132:133], v8 offset:12288\n"
    "s_waitcnt lgkmcnt(8)\n"
    "v_mfma_f32_16x16x32_bf16 a[0:3], v[72:75], a[216:219], a[0:3]\n"
    "ds_read_b64_tr_b16 v[134:135], v8 offset:12800\n"
    "ds_read_b128 a[232:235], v9 offset:33792\n"
    "v_mfma_f32_16x16x32_bf16 a[0:3], v[88:91], a[220:223], a[0:3]\n"
    "ds_read_b128 a[236:239], v9 offset:35072\n"
    "buffer_load_dwordx4 v2, s[44:47], s57 offen offset:1024 lds\n"
    "s_waitcnt lgkmcnt(7)\n"
    "v_mfma_f32_16x16x32_bf16 a[0:3], v[128:131], a[224:227], a[0:3]\n"
    "v_mfma_f32_16x16x32_bf16 a[16:19], v[128:131], a[228:231], a[16:19]\n"
    "v_mfma_f32_16x16x32_bf16 a[16:19], v[88:91], a[224:227], a[16:19]\n"
    "v_mfma_f32_16x16x32_bf16 a[16:19], v[72:75], a[220:223], a[16:19]\n"
    "buffer_load_dwordx4 v3, s[44:47], s57 offen offset:2048 lds\n"
    "v_mfma_f32_16x16x32_bf16 a[32:35], v[72:75], a[224:227], a[32:35]\n"
    "s_waitcnt vmcnt(12)\n"
    "ds_read_b128 a[240:243], v9 offset:36352\n"
    "v_mfma_f32_16x16x32_bf16 a[32:35], v[88:91], a[228:231], a[32:35]\n"
    "s_waitcnt vmcnt(11)\n"
    "ds_read_b128 a[244:247], v9 offset:37632\n"
    "s_waitcnt lgkmcnt(2)\n"
    "v_mfma_f32_16x16x32_bf16 a[32:35], v[128:131], a[232:235], a[32:35]\n"
    "v_mfma_f32_16x16x32_bf16 a[48:51], v[128:131], a[236:239], a[48:51]\n"
    "buffer_load_dwordx4 v4, s[44:47], s57 offen offset:3072 lds\n"
    "v_mfma_f32_16x16x32_bf16 a[48:51], v[88:91], a[232:235], a[48:51]\n"
    "v_mfma_f32_16x16x32_bf16 a[48:51], v[72:75], a[228:231], a[48:51]\n"
    "v_mfma_f32_16x16x32_bf16 a[64:67], v[72:75], a[232:235], a[64:67]\n"
    "ds_read_b128 a[248:251], v10 offset:28672\n"
    "s_mov_b32 m0, s68\n"
    "v_mfma_f32_16x16x32_bf16 a[64:67], v[88:91], a[236:239], a[64:67]\n"
    "ds_read_b128 a[252:255], v10 offset:29952\n"
    "buffer_load_dwordx4 v1, s[44:47], s58 offen offset:0 lds\n"
    "s_waitcnt lgkmcnt(2)\n"
    "v_mfma_f32_16x16x32_bf16 a[64:67], v[128:131], a[240:243], a[64:67]\n"
    "v_mfma_f32_16x16x32_bf16 a[80:83], v[128:131], a[244:247], a[80:83]\n"
    "v_mfma_f32_16x16x32_bf16 a[80:83], v[88:91], a[240:243], a[80:83]\n"
    "v_mfma_f32_16x16x32_bf16 a[80:83], v[72:75], a[236:239], a[80:83]\n"
    "buffer_load_dwordx4 v2, s[44:47], s58 offen offset:1024 lds\n"
    "v_mfma_f32_16x16x32_bf16 a[96:99], v[72:75], a[240:243], a[96:99]\n"
    "v_mfma_f32_16x16x32_bf16 a[96:99], v[88:91], a[244:247], a[96:99]\n"
    "s_waitcnt lgkmcnt(0)\n"
    "v_mfma_f32_16x16x32_bf16 a[96:99], v[128:131], a[248:251], a[96:99]\n"
    "ds_read_b128 a[128:131], v10 offset:31232\n"
    "v_mfma_f32_16x16x32_bf16 a[112:115], v[128:131], a[252:255], a[112:115]\n"
    "buffer_load_dwordx4 v3, s[44:47], s58 offen offset:2048 lds\n"
    "v_mfma_f32_16x16x32_bf16 a[112:115], v[88:91], a[248:251], a[112:115]\n"
    "v_mfma_f32_16x16x32_bf16 a[112:115], v[72:75], a[244:247], a[112:115]\n"
    "ds_read_b128 a[132:135], v10 offset:32512\n"
    "v_mfma_f32_16x16x32_bf16 a[4:7], v[76:79], a[216:219], a[4:7]\n"
    "v_mfma_f32_16x16x32_bf16 a[4:7], v[124:127], a[220:223], a[4:7]\n"
    "buffer_load_dwordx4 v4, s[44:47], s58 offen offset:3072 lds\n"
    "v_mfma_f32_16x16x32_bf16 a[4:7], v[112:115], a[224:227], a[4:7]\n"
    "ds_read_b128 a[140:143], v10 offset:33792\n"
    "v_mfma_f32_16x16x32_bf16 a[20:23], v[112:115], a[228:231], a[20:23]\n"
    "v_mfma_f32_16x16x32_bf16 a[20:23], v[124:127], a[224:227], a[20:23]\n"
    "s_mov_b32 m0, s69\n"
    "v_mfma_f32_16x16x32_bf16 a[20:23], v[76:79], a[220:223], a[20:23]\n"
    "L_st2_%=:\n"
    "s_cbranch_execz L_st6_%=\n"
    "ds_read_b128 a[144:147], v10 offset:35072\n"
    "buffer_load_dwordx4 v1, s[44:47], s59 offen offset:0 lds\n"
    "v_mfma_f32_16x16x32_bf16 a[36:39], v[76:79], a[224:227], a[36:39]\n"
    "v_mfma_f32_16x16x32_bf16 a[36:39], v[124:127], a[228:231], a[36:39]\n"
    "v_mfma_f32_16x16x32_bf16 a[36:39], v[112:115], a[232:235], a[36:39]\n"
    "ds_read_b128 a[148:151], v10 offset:36352\n"
    "v_mfma_f32_16x16x32_bf16 a[52:55], v[112:115], a[236:239], a[52:55]\n"
    "buffer_load_dwordx4 v2, s[44:47], s59 offen offset:1024 lds\n"
    "v_mfma_f32_16x16x32_bf16 a[52:55], v[124:127], a[232:235], a[52:55]\n"
    "v_mfma_f32_16x16x32_bf16 a[52:55], v[76:79], a[228:231], a[52:55]\n"
    "ds_read_b128 a[156:159], v10 offset:37632\n"
    "v_mfma_f32_16x16x32_bf16 a[68:71], v[76:79], a[232:235], a[68:71]\n"
    "v_mfma_f32_16x16x32_bf16 a[68:71], v[124:127], a[236:239], a[68:71]\n"
    "buffer_load_dwordx4 v3, s[44:47], s59 offen offset:2048 lds\n"
    "v_mfma_f32_16x16x32_bf16 a[68:71], v[112:115], a[240:243], a[68:71]\n"
    "ds_read_b128 a[160:163], v11 offset:28928\n"
    "v_mfma_f32_16x16x32_bf16 a[84:87], v[112:115], a[244:247], a[84:87]\n"
    "v_mfma_f32_16x16x32_bf16 a[84:87], v[124:127], a[240:243], a[84:87]\n"
    "v_mfma_f32_16x16x32_bf16 a[84:87], v[76:79], a[236:239], a[84:87]\n"
    "ds_read_b128 a[164:167], v11 offset:30208\n"
    "buffer_load_dwordx4 v4, s[44:47], s59 offen offset:3072 lds\n"
    "v_mfma_f32_16x16x32_bf16 a[100:103], v[76:79], a[240:243], a[100:103]\n"
    "v_mfma_f32_16x16x32_bf16 a[100:103], v[124:127], a[244:247], a[100:103]\n"
    "v_mfma_f32_16x16x32_bf16 a[100:103], v[112:115], a[248:251], a[100:103]\n"
    "ds_read_b128 a[136:139], v11 offset:31488\n"
    "s_mov_b32 m0, s70\n"
    "v_mfma_f32_16x16x32_bf16 a[116:119], v[112:115], a[252:255], a[116:119]\n"
    "buffer_load_dwordx4 v1, s[44:47], s60 offen offset:0 lds\n"
    "v_mfma_f32_16x16x32_bf16 a[116:119], v[124:127], a[248:251], a[116:119]\n"
    "v_mfma_f32_16x16x32_bf16 a[116:119], v[76:79], a[244:247], a[116:119]\n"
    "ds_read_b128 a[152:155], v11 offset:32768\n"
    "v_mfma_f32_16x16x32_bf16 a[8:11], v[80:83], a[216:219], a[8:11]\n"
    "v_mfma_f32_16x16x32_bf16 a[8:11], v[108:111], a[220:223], a[8:11]\n"
    "buffer_load_dwordx4 v2, s[44:47], s60 offen offset:1024 lds\n"
    "v_mfma_f32_16x16x32_bf16 a[8:11], v[96:99], a[224:227], a[8:11]\n"
    "ds_read_b128 a[168:171], v11 offset:34048\n"
    "v_mfma_f32_16x16x32_bf16 a[24:27], v[96:99], a[228:231], a[24:27]\n"
    "v_mfma_f32_16x16x32_bf16 a[24:27], v[108:111], a[224:227], a[24:27]\n"
    "v_mfma_f32_16x16x32_bf16 a[24:27], v[80:83], a[220:223], a[24:27]\n"
    "ds_read_b128 a[172:175], v11 offset:35328\n"
    "buffer_load_dwordx4 v3, s[44:47], s60 offen offset:2048 lds\n"
    "v_mfma_f32_16x16x32_bf16 a[40:43], v[80:83], a[224:227], a[40:43]\n"
    "v_mfma_f32_16x16x32_bf16 a[40:43], v[108:111], a[228:231], a[40:43]\n"
    "v_mfma_f32_16x16x32_bf16 a[40:43], v[96:99], a[232:235], a[40:43]\n"
    "v_mfma_f32_16x16x32_bf16 a[56:59], v[96:99], a[236:239], a[56:59]\n"
    "buffer_load_dwordx4 v4, s[44:47], s60 offen offset:3072 lds\n"
    "v_mfma_f32_16x16x32_bf16 a[56:59], v[108:111], a[232:235], a[56:59]\n"
    "v_mfma_f32_16x16x32_bf16 a[56:59], v[80:83], a[228:231], a[56:59]\n"
    "v_mfma_f32_16x16x32_bf16 a[72:75], v[80:83], a[232:235], a[72:75]\n"
    "s_waitcnt vmcnt(20)\n"
    "ds_read_b64_tr_b16 v[116:117], v5 offset:16384\n"
    "s_mov_b32 m0, s71\n"
    "v_mfma_f32_16x16x32_bf16 a[72:75], v[108:111], a[236:239], a[72:75]\n"
    "ds_read_b64_tr_b16 v[118:119], v5 offset:16896\n"
    "buffer_load_dwordx4 v1, s[44:47], s61 offen offset:0 lds\n"
    "v_mfma_f32_16x16x32_bf16 a[72:75], v[96:99], a[240:243], a[72:75]\n"
    "ds_read_b64_tr_b16 v[100:101], v6 offset:16384\n"
    "v_mfma_f32_16x16x32_bf16 a[88:91], v[96:99], a[244:247], a[88:91]\n"
    "ds_read_b64_tr_b16 v[102:103], v6 offset:16896\n"
    "v_mfma_f32_16x16x32_bf16 a[88:91], v[108:111], a[240:243], a[88:91]\n"
    "ds_read_b64_tr_b16 v[136:137], v7 offset:16384\n"
    "v_mfma_f32_16x16x32_bf16 a[88:91], v[80:83], a[236:239], a[88:91]\n"
    "ds_read_b64_tr_b16 v[138:139], v7 offset:16896\n"
    "buffer_load_dwordx4 v2, s[44:47], s61 offen offset:1024 lds\n"
    "v_mfma_f32_16x16x32_bf16 a[104:107], v[80:83], a[240:243], a[104:107]\n"
    "ds_read_b64_tr_b16 v[120:121], v8 offset:16384\n"
    "v_mfma_f32_16x16x32_bf16 a[104:107], v[108:111], a[244:247], a[104:107]\n"
    "ds_read_b64_tr_b16 v[122:123], v8 offset:16896\n"
    "v_mfma_f32_16x16x32_bf16 a[104:107], v[96:99], a[248:251], a[104:107]\n"
    "v_mfma_f32_16x16x32_bf16 a[120:123], v[96:99], a[252:255], a[120:123]\n"
    "buffer_load_dwordx4 v3, s[44:47], s61 offen offset:2048 lds\n"
    "v_mfma_f32_16x16x32_bf16 a[120:123], v[108:111], a[248:251], a[120:123]\n"
    "v_mfma_f32_16x16x32_bf16 a[120:123], v[80:83], a[244:247], a[120:123]\n"
    "v_mfma_f32_16x16x32_bf16 a[12:15], v[84:87], a[216:219], a[12:15]\n"
    "v_mfma_f32_16x16x32_bf16 a[12:15], v[92:95], a[220:223], a[12:15]\n"
    "buffer_load_dwordx4 v4, s[44:47], s61 offen offset:3072 lds\n"
    "v_mfma_f32_16x16x32_bf16 a[12:15], v[132:135], a[224:227], a[12:15]\n"
    "v_mfma_f32_16x16x32_bf16 a[28:31], v[132:135], a[228:231], a[28:31]\n"
    "v_mfma_f32_16x16x32_bf16 a[28:31], v[92:95], a[224:227], a[28:31]\n"
    "s_waitcnt vmcnt(20)\n"
    "ds_read_b64_tr_b16 v[104:105], v5 offset:20480\n"
    "s_mov_b32 m0, s72\n"
    "v_mfma_f32_16x16x32_bf16 a[28:31], v[84:87], a[220:223], a[28:31]\n"
    "ds_read_b64_tr_b16 v[106:107], v5 offset:20992\n"
    "s_waitcnt lgkmcnt(2)\n"
    "buffer_load_dwordx4 v1, s[44:47], s62 offen offset:0 lds\n"
    "v_mfma_f32_16x16x32_bf16 a[44:47], v[84:87], a[224:227], a[44:47]\n"
    "ds_read_b64_tr_b16 v[172:173], v6 offset:20480\n"
    "v_mfma_f32_16x16x32_bf16 a[44:47], v[92:95], a[228:231], a[44:47]\n"
    "ds_read_b64_tr_b16 v[174:175], v6 offset:20992\n"
    "v_mfma_f32_16x16x32_bf16 a[44:47], v[132:135], a[232:235], a[44:47]\n"
    "ds_read_b64_tr_b16 v[156:157], v7 offset:20480\n"
    "v_mfma_f32_16x16x32_bf16 a[60:63], v[132:135], a[236:239], a[60:63]\n"
    "ds_read_b64_tr_b16 v[158:159], v7 offset:20992\n"
    "buffer_load_dwordx4 v2, s[44:47], s62 offen offset:1024 lds\n"
    "v_mfma_f32_16x16x32_bf16 a[60:63], v[92:95], a[232:235], a[60:63]\n"
    "ds_read_b64_tr_b16 v[140:141], v8 offset:20480\n"
    "v_mfma_f32_16x16x32_bf16 a[60:63], v[84:87], a[228:231], a[60:63]\n"
    "ds_read_b64_tr_b16 v[142:143], v8 offset:20992\n"
    "v_mfma_f32_16x16x32_bf16 a[76:79], v[84:87], a[232:235], a[76:79]\n"
    "v_mfma_f32_16x16x32_bf16 a[76:79], v[92:95], a[236:239], a[76:79]\n"
    "buffer_load_dwordx4 v3, s[44:47], s62 offen offset:2048 lds\n"
    "v_mfma_f32_16x16x32_bf16 a[76:79], v[132:135], a[240:243], a[76:79]\n"
    "v_mfma_f32_16x16x32_bf16 a[92:95], v[132:135], a[244:247], a[92:95]\n"
    "v_mfma_f32_16x16x32_bf16 a[92:95], v[92:95], a[240:243], a[92:95]\n"
    "v_mfma_f32_16x16x32_bf16 a[92:95], v[84:87], a[236:239], a[92:95]\n"
    "s_waitcnt vmcnt(19)\n"
    "ds_read_b64_tr_b16 v[176:177], v5 offset:24576\n"
    "buffer_load_dwordx4 v4, s[44:47], s62 offen offset:3072 lds\n"
    "v_mfma_f32_16x16x32_bf16 a[108:111], v[84:87], a[240:243], a[108:111]\n"
    "ds_read_b64_tr_b16 v[178:179], v5 offset:25088\n"
    "v_mfma_f32_16x16x32_bf16 a[108:111], v[92:95], a[244:247], a[108:111]\n"
    "ds_read_b64_tr_b16 v[160:161], v6 offset:24576\n"
    "v_mfma_f32_16x16x32_bf16 a[108:111], v[132:135], a[248:251], a[108:111]\n"
    "ds_read_b64_tr_b16 v[162:163], v6 offset:25088\n"
    "s_mov_b32 m0, s73\n"
    "v_mfma_f32_16x16x32_bf16 a[124:127], v[132:135], a[252:255], a[124:127]\n"
    "ds_read_b64_tr_b16 v[144:145], v7 offset:24576\n"
    "s_waitcnt lgkmcnt(5)\n"
    "buffer_load_dwordx4 v1, s[44:47], s63 offen offset:0 lds\n"
    "v_mfma_f32_16x16x32_bf16 a[124:127], v[92:95], a[248:251], a[124:127]\n"
    "ds_read_b64_tr_b16 v[146:147], v7 offset:25088\n"
    "v_mfma_f32_16x16x32_bf16 a[124:127], v[84:87], a[244:247], a[124:127]\n"
    "ds_read_b64_tr_b16 v[180:181], v8 offset:24576\n"
    "v_mfma_f32_16x16x32_bf16 a[0:3], v[116:119], a[228:231], a[0:3]\n"
    "ds_read_b64_tr_b16 v[182:183], v8 offset:25088\n"
    "v_mfma_f32_16x16x32_bf16 a[0:3], v[104:107], a[232:235], a[0:3]\n"
    "buffer_load_dwordx4 v2, s[44:47], s63 offen offset:1024 lds\n"
    "s_waitcnt lgkmcnt(5)\n"
    "v_mfma_f32_16x16x32_bf16 a[0:3], v[176:179], a[236:239], a[0:3]\n"
    "v_mfma_f32_16x16x32_bf16 a[16:19], v[176:179], a[240:243], a[16:19]\n"
    "v_mfma_f32_16x16x32_bf16 a[16:19], v[104:107], a[236:239], a[16:19]\n"
    "v_mfma_f32_16x16x32_bf16 a[16:19], v[116:119], a[232:235], a[16:19]\n"
    "buffer_load_dwordx4 v3, s[44:47], s63 offen offset:2048 lds\n"
    "v_mfma_f32_16x16x32_bf16 a[32:35], v[116:119], a[236:239], a[32:35]\n"
    "v_mfma_f32_16x16x32_bf16 a[32:35], v[104:107], a[240:243], a[32:35]\n"
    "v_mfma_f32_16x16x32_bf16 a[32:35], v[176:179], a[244:247], a[32:35]\n"
    "v_mfma_f32_16x16x32_bf16 a[48:51], v[176:179], a[248:251], a[48:51]\n"
    "buffer_load_dwordx4 v4, s[44:47], s63 offen offset:3072 lds\n"
    "v_mfma_f32_16x16x32_bf16 a[48:51], v[104:107], a[244:247], a[48:51]\n"
    "v_mfma_f32_16x16x32_bf16 a[48:51], v[116:119], a[240:243], a[48:51]\n"
    "v_mfma_f32_16x16x32_bf16 a[64:67], v[116:119], a[244:247], a[64:67]\n"
    "s_waitcnt vmcnt(20)\n"
    "ds_read_b64_tr_b16 v[164:165], v5 offset:0\n"
    "s_mov_b32 m0, s74\n"
    "v_mfma_f32_16x16x32_bf16 a[64:67], v[104:107], a[248:251], a[64:67]\n"
    "ds_read_b64_tr_b16 v[166:167], v5 offset:512\n"
    "s_waitcnt lgkmcnt(2)\n"
    "buffer_load_dwordx4 v1, s[44:47], s64 offen offset:0 lds\n"
    "v_mfma_f32_16x16x32_bf16 a[64:67], v[176:179], a[252:255], a[64:67]\n"
    "ds_read_b64_tr_b16 v[148:149], v6 offset:0\n"
    "v_mfma_f32_16x16x32_bf16 a[80:83], v[176:179], a[128:131], a[80:83]\n"
    "ds_read_b64_tr_b16 v[150:151], v6 offset:512\n"
    "v_mfma_f32_16x16x32_bf16 a[80:83], v[104:107], a[252:255], a[80:83]\n"
    "ds_read_b64_tr_b16 v[184:185], v7 offset:0\n"
    "v_mfma_f32_16x16x32_bf16 a[80:83], v[116:119], a[248:251], a[80:83]\n"
    "ds_read_b64_tr_b16 v[186:187], v7 offset:512\n"
    "buffer_load_dwordx4 v2, s[44:47], s64 offen offset:1024 lds\n"
    "v_mfma_f32_16x16x32_bf16 a[96:99], v[116:119], a[252:255], a[96:99]\n"
    "ds_read_b64_tr_b16 v[168:169], v8 offset:0\n"
    "v_mfma_f32_16x16x32_bf16 a[96:99], v[104:107], a[128:131], a[96:99]\n"
    "ds_read_b64_tr_b16 v[170:171], v8 offset:512\n"
    "v_mfma_f32_16x16x32_bf16 a[96:99], v[176:179], a[132:135], a[96:99]\n"
    "v_mfma_f32_16x16x32_bf16 a[112:115], v[176:179], a[140:143], a[112:115]\n"
    "buffer_load_dwordx4 v3, s[44:47], s64 offen offset:2048 lds\n"
    "v_mfma_f32_16x16x32_bf16 a[112:115], v[104:107], a[132:135], a[112:115]\n"
    "v_mfma_f32_16x16x32_bf16 a[112:115], v[116:119], a[128:131], a[112:115]\n"
    "v_mfma_f32_16x16x32_bf16 a[4:7], v[100:103], a[228:231], a[4:7]\n"
    "v_mfma_f32_16x16x32_bf16 a[4:7], v[172:175], a[232:235], a[4:7]\n"
    "buffer_load_dwordx4 v4, s[44:47], s64 offen offset:3072 lds\n"
    "v_mfma_f32_16x16x32_bf16 a[4:7], v[160:163], a[236:239], a[4:7]\n"
    "v_mfma_f32_16x16x32_bf16 a[20:23], v[160:163], a[240:243], a[20:23]\n"
    "v_mfma_f32_16x16x32_bf16 a[20:23], v[172:175], a[236:239], a[20:23]\n"
    "s_waitcnt vmcnt(20)\n"
    "ds_read_b64_tr_b16 v[152:153], v5 offset:4096\n"
    "s_mov_b32 m0, s68\n"
    "v_mfma_f32_16x16x32_bf16 a[20:23], v[100:103], a[232:235], a[20:23]\n"
    "ds_read_b64_tr_b16 v[154:155], v5 offset:4608\n"
    "s_waitcnt lgkmcnt(2)\n"
    "buffer_load_dwordx4 v1, s[44:47], s65 offen offset:0 lds\n"
    "v_mfma_f32_16x16x32_bf16 a[36:39], v[100:103], a[236:239], a[36:39]\n"
    "ds_read_b64_tr_b16 v[220:221], v6 offset:4096\n"
    "v_mfma_f32_16x16x32_bf16 a[36:39], v[172:175], a[240:243], a[36:39]\n"
    "ds_read_b64_tr_b16 v[222:223], v6 offset:4608\n"
    "v_mfma_f32_16x16x32_bf16 a[36:39], v[160:163], a[244:247], a[36:39]\n"
    "ds_read_b64_tr_b16 v[204:205], v7 offset:4096\n"
    "v_mfma_f32_16x16x32_bf16 a[52:55], v[160:163], a[248:251], a[52:55]\n"
    "ds_read_b64_tr_b16 v[206:207], v7 offset:4608\n"
    "buffer_load_dwordx4 v2, s[44:47], s65 offen offset:1024 lds\n"
    "v_mfma_f32_16x16x32_bf16 a[52:55], v[172:175], a[244:247], a[52:55]\n"
    "ds_read_b64_tr_b16 v[188:189], v8 offset:4096\n"
    "v_mfma_f32_16x16x32_bf16 a[52:55], v[100:103], a[240:243], a[52:55]\n"
    "ds_read_b64_tr_b16 v[190:191], v8 offset:4608\n"
    "v_mfma_f32_16x16x32_bf16 a[68:71], v[100:103], a[244:247], a[68:71]\n"
    "v_mfma_f32_16x16x32_bf16 a[68:71], v[172:175], a[248:251], a[68:71]\n"
    "buffer_load_dwordx4 v3, s[44:47], s65 offen offset:2048 lds\n"
    "v_mfma_f32_16x16x32_bf16 a[68:71], v[160:163], a[252:255], a[68:71]\n"
    "v_mfma_f32_16x16x32_bf16 a[84:87], v[160:163], a[128:131], a[84:87]\n"
    "v_mfma_f32_16x16x32_bf16 a[84:87], v[172:175], a[252:255], a[84:87]\n"
    "v_mfma_f32_16x16x32_bf16 a[84:87], v[100:103], a[248:251], a[84:87]\n"
    "buffer_load_dwordx4 v4, s[44:47], s65 offen offset:3072 lds\n"
    "v_mfma_f32_16x16x32_bf16 a[100:103], v[100:103], a[252:255], a[100:103]\n"
    "v_mfma_f32_16x16x32_bf16 a[100:103], v[172:175], a[128:131], a[100:103]\n"
    "v_mfma_f32_16x16x32_bf16 a[100:103], v[160:163], a[132:135], a[100:103]\n"
    "s_mov_b32 m0, s69\n"
    "v_mfma_f32_16x16x32_bf16 a[116:119], v[160:163], a[140:143], a[116:119]\n"
    "s_waitcnt lgkmcnt(0)\n"
    "buffer_load_dwordx4 v1, s[44:47], s66 offen offset:0 lds\n"
    "v_mfma_f32_16x16x32_bf16 a[116:119], v[172:175], a[132:135], a[116:119]\n"
    "v_mfma_f32_16x16x32_bf16 a[116:119], v[100:103], a[128:131], a[116:119]\n"
    "v_mfma_f32_16x16x32_bf16 a[8:11], v[136:139], a[228:231], a[8:11]\n"
    "v_mfma_f32_16x16x32_bf16 a[8:11], v[156:159], a[232:235], a[8:11]\n"
    "buffer_load_dwordx4 v2, s[44:47], s66 offen offset:1024 lds\n"
    "v_mfma_f32_16x16x32_bf16 a[8:11], v[144:147], a[236:239], a[8:11]\n"
    "v_mfma_f32_16x16x32_bf16 a[24:27], v[144:147], a[240:243], a[24:27]\n"
    "v_mfma_f32_16x16x32_bf16 a[24:27], v[156:159], a[236:239], a[24:27]\n"
    "v_mfma_f32_16x16x32_bf16 a[24:27], v[136:139], a[232:235], a[24:27]\n"
    "buffer_load_dwordx4 v3, s[44:47], s66 offen offset:2048 lds\n"
    "v_mfma_f32_16x16x32_bf16 a[40:43], v[136:139], a[236:239], a[40:43]\n"
    "v_mfma_f32_16x16x32_bf16 a[40:43], v[156:159], a[240:243], a[40:43]\n"
    "v_mfma_f32_16x16x32_bf16 a[40:43], v[144:147], a[244:247], a[40:43]\n"
    "v_mfma_f32_16x16x32_bf16 a[56:59], v[144:147], a[248:251], a[56:59]\n"
    "buffer_load_dwordx4 v4, s[44:47], s66 offen offset:3072 lds\n"
    "v_mfma_f32_16x16x32_bf16 a[56:59], v[156:159], a[244:247], a[56:59]\n"
    "v_mfma_f32_16x16x32_bf16 a[56:59], v[136:139], a[240:243], a[56:59]\n"
    "v_mfma_f32_16x16x32_bf16 a[72:75], v[136:139], a[244:247], a[72:75]\n"
    "v_mfma_f32_16x16x32_bf16 a[72:75], v[156:159], a[248:251], a[72:75]\n"
    "v_mfma_f32_16x16x32_bf16 a[72:75], v[144:147], a[252:255], a[72:75]\n"
    "v_mfma_f32_16x16x32_bf16 a[88:91], v[144:147], a[128:131], a[88:91]\n"
    "v_mfma_f32_16x16x32_bf16 a[88:91], v[156:159], a[252:255], a[88:91]\n"
    "v_mfma_f32_16x16x32_bf16 a[88:91], v[136:139], a[248:251], a[88:91]\n"
    "global_load_dword v23, v25, s[80:81] sc1\n"
    "v_mfma_f32_16x16x32_bf16 a[104:107], v[136:139], a[252:255], a[104:107]\n"
    "v_mfma_f32_16x16x32_bf16 a[104:107], v[156:159], a[128:131], a[104:107]\n"
    "buffer_load_dword v23, v33, s[48:51], 0 offen\n"
    "v_mfma_f32_16x16x32_bf16 a[104:107], v[144:147], a[132:135], a[104:107]\n"
    "v_add_u32 v22, 262144, v22\n"
    "v_mfma_f32_16x16x32_bf16 a[120:123], v[144:147], a[140:143], a[120:123]\n"
    "buffer_load_dword v23, v22, s[44:47], s52 offen\n"
    "v_mfma_f32_16x16x32_bf16 a[120:123], v[156:159], a[132:135], a[120:123]\n"
    "v_mfma_f32_16x16x32_bf16 a[120:123], v[136:139], a[128:131], a[120:123]\n"
    "v_mfma_f32_16x16x32_bf16 a[12:15], v[120:123], a[228:231], a[12:15]\n"
    "v_mfma_f32_16x16x32_bf16 a[12:15], v[140:143], a[232:235], a[12:15]\n"
    "buffer_load_dword v23, v22, s[44:47], s59 offen\n"
    "v_mfma_f32_16x16x32_bf16 a[12:15], v[180:183], a[236:239], a[12:15]\n"
    "v_mfma_f32_16x16x32_bf16 a[28:31], v[180:183], a[240:243], a[28:31]\n"
    "v_mfma_f32_16x16x32_bf16 a[28:31], v[140:143], a[236:239], a[28:31]\n"
    "v_mfma_f32_16x16x32_bf16 a[28:31], v[120:123], a[232:235], a[28:31]\n"
    "v_mfma_f32_16x16x32_bf16 a[44:47], v[120:123], a[236:239], a[44:47]\n"
    "v_mfma_f32_16x16x32_bf16 a[44:47], v[140:143], a[240:243], a[44:47]\n"
    "v_mfma_f32_16x16x32_bf16 a[44:47], v[180:183], a[244:247], a[44:47]\n"
    "v_mfma_f32_16x16x32_bf16 a[60:63], v[180:183], a[248:251], a[60:63]\n"
    "v_mfma_f32_16x16x32_bf16 a[60:63], v[140:143], a[244:247], a[60:63]\n"
    "v_mfma_f32_16x16x32_bf16 a[60:63], v[120:123], a[240:243], a[60:63]\n"
    "v_mfma_f32_16x16x32_bf16 a[76:79], v[120:123], a[244:247], a[76:79]\n"
    "v_mfma_f32_16x16x32_bf16 a[76:79], v[140:143], a[248:251], a[76:79]\n"
    "buffer_load_dword v23, v40, s[40:43], 0 offen offset:512\n"
    "v_mfma_f32_16x16x32_bf16 a[76:79], v[180:183], a[252:255], a[76:79]\n"
    "v_mfma_f32_16x16x32_bf16 a[92:95], v[180:183], a[128:131], a[92:95]\n"
    "v_mfma_f32_16x16x32_bf16 a[92:95], v[140:143], a[252:255], a[92:95]\n"
    "v_mfma_f32_16x16x32_bf16 a[92:95], v[120:123], a[248:251], a[92:95]\n"
    "s_waitcnt vmcnt(29)\n"
    "ds_read_b64_tr_b16 v[224:225], v5 offset:8192\n"
    "v_mfma_f32_16x16x32_bf16 a[108:111], v[120:123], a[252:255], a[108:111]\n"
    "ds_read_b64_tr_b16 v[226:227], v5 offset:8704\n"
    "v_mfma_f32_16x16x32_bf16 a[108:111], v[140:143], a[128:131], a[108:111]\n"
    "ds_read_b64_tr_b16 v[208:209], v6 offset:8192\n"
    "v_mfma_f32_16x16x32_bf16 a[108:111], v[180:183], a[132:135], a[108:111]\n"
    "ds_read_b64_tr_b16 v[210:211], v6 offset:8704\n"
    "v_mfma_f32_16x16x32_bf16 a[124:127], v[180:183], a[140:143], a[124:127]\n"
    "ds_read_b64_tr_b16 v[192:193], v7 offset:8192\n"
    "v_mfma_f32_16x16x32_bf16 a[124:127], v[140:143], a[132:135], a[124:127]\n"
    "ds_read_b64_tr_b16 v[194:195], v7 offset:8704\n"
    "v_mfma_f32_16x16x32_bf16 a[124:127], v[120:123], a[128:131], a[124:127]\n"
    "ds_read_b64_tr_b16 v[228:229], v8 offset:8192\n"
    "v_mfma_f32_16x16x32_bf16 a[0:3], v[164:167], a[240:243], a[0:3]\n"
    "ds_read_b64_tr_b16 v[230:231], v8 offset:8704\n"
    "v_mfma_f32_16x16x32_bf16 a[0:3], v[152:155], a[244:247], a[0:3]\n"
    "s_waitcnt lgkmcnt(5)\n"
    "v_mfma_f32_16x16x32_bf16 a[0:3], v[224:227], a[248:251], a[0:3]\n"
    "v_mfma_f32_16x16x32_bf16 a[16:19], v[224:227], a[252:255], a[16:19]\n"
    "v_mfma_f32_16x16x32_bf16 a[16:19], v[152:155], a[248:251], a[16:19]\n"
    "v_mfma_f32_16x16x32_bf16 a[16:19], v[164:167], a[244:247], a[16:19]\n"
    "v_mfma_f32_16x16x32_bf16 a[32:35], v[164:167], a[248:251], a[32:35]\n"
    "v_mfma_f32_16x16x32_bf16 a[32:35], v[152:155], a[252:255], a[32:35]\n"
    "v_mfma_f32_16x16x32_bf16 a[32:35], v[224:227], a[128:131], a[32:35]\n"
    "v_mfma_f32_16x16x32_bf16 a[48:51], v[224:227], a[132:135], a[48:51]\n"
    "v_mfma_f32_16x16x32_bf16 a[48:51], v[152:155], a[128:131], a[48:51]\n"
    "v_mfma_f32_16x16x32_bf16 a[48:51], v[164:167], a[252:255], a[48:51]\n"
    "v_mfma_f32_16x16x32_bf16 a[64:67], v[164:167], a[128:131], a[64:67]\n"
    "v_mfma_f32_16x16x32_bf16 a[64:67], v[152:155], a[132:135], a[64:67]\n"
    "v_mfma_f32_16x16x32_bf16 a[64:67], v[224:227], a[140:143], a[64:67]\n"
    "v_mfma_f32_16x16x32_bf16 a[80:83], v[224:227], a[144:147], a[80:83]\n"
    "v_mfma_f32_16x16x32_bf16 a[80:83], v[152:155], a[140:143], a[80:83]\n"
    "v_mfma_f32_16x16x32_bf16 a[80:83], v[164:167], a[132:135], a[80:83]\n"
    "v_mfma_f32_16x16x32_bf16 a[96:99], v[164:167], a[140:143], a[96:99]\n"
    "v_mfma_f32_16x16x32_bf16 a[96:99], v[152:155], a[144:147], a[96:99]\n"
    "v_mfma_f32_16x16x32_bf16 a[96:99], v[224:227], a[148:151], a[96:99]\n"
    "v_mfma_f32_16x16x32_bf16 a[112:115], v[224:227], a[156:159], a[112:115]\n"
    "v_mfma_f32_16x16x32_bf16 a[112:115], v[152:155], a[148:151], a[112:115]\n"
    "v_mfma_f32_16x16x32_bf16 a[112:115], v[164:167], a[144:147], a[112:115]\n"
    "v_mfma_f32_16x16x32_bf16 a[4:7], v[148:151], a[240:243], a[4:7]\n"
    "v_mfma_f32_16x16x32_bf16 a[4:7], v[220:223], a[244:247], a[4:7]\n"
    "s_waitcnt lgkmcnt(0)\n"
    "v_mfma_f32_16x16x32_bf16 a[4:7], v[208:211], a[248:251], a[4:7]\n"
    "v_mfma_f32_16x16x32_bf16 a[20:23], v[208:211], a[252:255], a[20:23]\n"
    "v_mfma_f32_16x16x32_bf16 a[20:23], v[220:223], a[248:251], a[20:23]\n"
    "v_mfma_f32_16x16x32_bf16 a[20:23], v[148:151], a[244:247], a[20:23]\n"
    "v_mfma_f32_16x16x32_bf16 a[36:39], v[148:151], a[248:251], a[36:39]\n"
    "v_mfma_f32_16x16x32_bf16 a[36:39], v[220:223], a[252:255], a[36:39]\n"
    "v_mfma_f32_16x16x32_bf16 a[36:39], v[208:211], a[128:131], a[36:39]\n"
    "v_mfma_f32_16x16x32_bf16 a[52:55], v[208:211], a[132:135], a[52:55]\n"
    "v_mfma_f32_16x16x32_bf16 a[52:55], v[220:223], a[128:131], a[52:55]\n"
    "v_mfma_f32_16x16x32_bf16 a[52:55], v[148:151], a[252:255], a[52:55]\n"
    "v_mfma_f32_16x16x32_bf16 a[68:71], v[148:151], a[128:131], a[68:71]\n"
    "v_mfma_f32_16x16x32_bf16 a[68:71], v[220:223], a[132:135], a[68:71]\n"
    "v_mfma_f32_16x16x32_bf16 a[68:71], v[208:211], a[140:143], a[68:71]\n"
    "v_mfma_f32_16x16x32_bf16 a[84:87], v[208:211], a[144:147], a[84:87]\n"
    "v_mfma_f32_16x16x32_bf16 a[84:87], v[220:223], a[140:143], a[84:87]\n"
    "v_mfma_f32_16x16x32_bf16 a[84:87], v[148:151], a[132:135], a[84:87]\n"
    "v_mfma_f32_16x16x32_bf16 a[100:103], v[148:151], a[140:143], a[100:103]\n"
    "v_mfma_f32_16x16x32_bf16 a[100:103], v[220:223], a[144:147], a[100:103]\n"
    "v_mfma_f32_16x16x32_bf16 a[100:103], v[208:211], a[148:151], a[100:103]\n"
    "v_mfma_f32_16x16x32_bf16 a[116:119], v[208:211], a[156:159], a[116:119]\n"
    "v_mfma_f32_16x16x32_bf16 a[116:119], v[220:223], a[148:151], a[116:119]\n"
    "v_mfma_f32_16x16x32_bf16 a[116:119], v[148:151], a[144:147], a[116:119]\n"
    "v_mfma_f32_16x16x32_bf16 a[8:11], v[184:187], a[240:243], a[8:11]\n"
    "v_mfma_f32_16x16x32_bf16 a[8:11], v[204:207], a[244:247], a[8:11]\n"
    "v_mfma_f32_16x16x32_bf16 a[8:11], v[192:195], a[248:251], a[8:11]\n"
    "v_mfma_f32_16x16x32_bf16 a[24:27], v[192:195], a[252:255], a[24:27]\n"
    "v_mfma_f32_16x16x32_bf16 a[24:27], v[204:207], a[248:251], a[24:27]\n"
    "v_mfma_f32_16x16x32_bf16 a[24:27], v[184:187], a[244:247], a[24:27]\n"
    "v_mfma_f32_16x16x32_bf16 a[40:43], v[184:187], a[248:251], a[40:43]\n"
    "v_mfma_f32_16x16x32_bf16 a[40:43], v[204:207], a[252:255], a[40:43]\n"
    "v_mfma_f32_16x16x32_bf16 a[40:43], v[192:195], a[128:131], a[40:43]\n"
    "v_mfma_f32_16x16x32_bf16 a[56:59], v[192:195], a[132:135], a[56:59]\n"
    "v_mfma_f32_16x16x32_bf16 a[56:59], v[204:207], a[128:131], a[56:59]\n"
    "v_mfma_f32_16x16x32_bf16 a[56:59], v[184:187], a[252:255], a[56:59]\n"
    "v_mfma_f32_16x16x32_bf16 a[72:75], v[184:187], a[128:131], a[72:75]\n"
    "v_mfma_f32_16x16x32_bf16 a[72:75], v[204:207], a[132:135], a[72:75]\n"
    "v_mfma_f32_16x16x32_bf16 a[72:75], v[192:195], a[140:143], a[72:75]\n"
    "v_mfma_f32_16x16x32_bf16 a[88:91], v[192:195], a[144:147], a[88:91]\n"
    "v_mfma_f32_16x16x32_bf16 a[88:91], v[204:207], a[140:143], a[88:91]\n"
    "v_mfma_f32_16x16x32_bf16 a[88:91], v[184:187], a[132:135], a[88:91]\n"
    "v_mfma_f32_16x16x32_bf16 a[104:107], v[184:187], a[140:143], a[104:107]\n"
    "v_mfma_f32_16x16x32_bf16 a[104:107], v[204:207], a[144:147], a[104:107]\n"
    "v_mfma_f32_16x16x32_bf16 a[104:107], v[192:195], a[148:151], a[104:107]\n"
    "v_mfma_f32_16x16x32_bf16 a[120:123], v[192:195], a[156:159], a[120:123]\n"
    "v_mfma_f32_16x16x32_bf16 a[120:123], v[204:207], a[148:151], a[120:123]\n"
    "v_mfma_f32_16x16x32_bf16 a[120:123], v[184:187], a[144:147], a[120:123]\n"
    "s_waitcnt vmcnt(25)\n"
    "ds_read_b64_tr_b16 v[212:213], v5 offset:12288\n"
    "v_mfma_f32_16x16x32_bf16 a[12:15], v[168:171], a[240:243], a[12:15]\n"
    "ds_read_b64_tr_b16 v[214:215], v5 offset:12800\n"
    "v_mfma_f32_16x16x32_bf16 a[12:15], v[188:191], a[244:247], a[12:15]\n"
    "ds_read_b64_tr_b16 v[196:197], v6 offset:12288\n"
    "global_load_dwordx4 v[232:235], v24, s[76:77] offset:0 sc1\n"
    "v_mfma_f32_16x16x32_bf16 a[12:15], v[228:231], a[248:251], a[12:15]\n"
    "ds_read_b64_tr_b16 v[198:199], v6 offset:12800\n"
    "v_mfma_f32_16x16x32_bf16 a[28:31], v[228:231], a[252:255], a[28:31]\n"
    "ds_read_b64_tr_b16 v[216:217], v7 offset:12288\n"
    "global_load_dwordx4 v[200:203], v24, s[76:77] offset:1024 sc1\n"
    "v_mfma_f32_16x16x32_bf16 a[28:31], v[188:191], a[248:251], a[28:31]\n"
    "ds_read_b64_tr_b16 v[218:219], v7 offset:12800\n"
    "v_mfma_f32_16x16x32_bf16 a[28:31], v[168:171], a[244:247], a[28:31]\n"
    "ds_read_b64_tr_b16 v[56:57], v8 offset:12288\n"
    "global_load_dwordx4 v[252:255], v24, s[76:77] offset:2048 sc1\n"
    "v_mfma_f32_16x16x32_bf16 a[44:47], v[168:171], a[248:251], a[44:47]\n"
    "ds_read_b64_tr_b16 v[58:59], v8 offset:12800\n"
    "v_mfma_f32_16x16x32_bf16 a[44:47], v[188:191], a[252:255], a[44:47]\n"
    "global_load_dwordx4 v[236:239], v24, s[76:77] offset:3072 sc1\n"
    "v_mfma_f32_16x16x32_bf16 a[44:47], v[228:231], a[128:131], a[44:47]\n"
    "s_waitcnt vmcnt(25)\n"
    "ds_read_b64_tr_b16 v[60:61], v5 offset:16384\n"
    "v_mfma_f32_16x16x32_bf16 a[60:63], v[228:231], a[132:135], a[60:63]\n"
    "ds_read_b64_tr_b16 v[62:63], v5 offset:16896\n"
    "v_mfma_f32_16x16x32_bf16 a[60:63], v[188:191], a[128:131], a[60:63]\n"
    "ds_read_b64_tr_b16 v[44:45], v6 offset:16384\n"
    "v_mfma_f32_16x16x32_bf16 a[60:63], v[168:171], a[252:255], a[60:63]\n"
    "ds_read_b64_tr_b16 v[46:47], v6 offset:16896\n"
    "v_mfma_f32_16x16x32_bf16 a[76:79], v[168:171], a[128:131], a[76:79]\n"
    "ds_read_b64_tr_b16 v[240:241], v7 offset:16384\n"
    "v_mfma_f32_16x16x32_bf16 a[76:79], v[188:191], a[132:135], a[76:79]\n"
    "ds_read_b64_tr_b16 v[242:243], v7 offset:16896\n"
    "v_mfma_f32_16x16x32_bf16 a[76:79], v[228:231], a[140:143], a[76:79]\n"
    "ds_read_b64_tr_b16 v[64:65], v8 offset:16384\n"
    "v_mfma_f32_16x16x32_bf16 a[92:95], v[228:231], a[144:147], a[92:95]\n"
    "ds_read_b64_tr_b16 v[66:67], v8 offset:16896\n"
    "v_mfma_f32_16x16x32_bf16 a[92:95], v[188:191], a[140:143], a[92:95]\n"
    "v_mfma_f32_16x16x32_bf16 a[92:95], v[168:171], a[132:135], a[92:95]\n"
    "s_waitcnt vmcnt(21)\n"
    "ds_read_b64_tr_b16 v[48:49], v5 offset:20480\n"
    "v_mfma_f32_16x16x32_bf16 a[108:111], v[168:171], a[140:143], a[108:111]\n"
    "ds_read_b64_tr_b16 v[50:51], v5 offset:20992\n"
    "v_mfma_f32_16x16x32_bf16 a[108:111], v[188:191], a[144:147], a[108:111]\n"
    "ds_read_b64_tr_b16 v[244:245], v6 offset:20480\n"
    "v_mfma_f32_16x16x32_bf16 a[108:111], v[228:231], a[148:151], a[108:111]\n"
    "ds_read_b64_tr_b16 v[246:247], v6 offset:20992\n"
    "v_mfma_f32_16x16x32_bf16 a[124:127], v[228:231], a[156:159], a[124:127]\n"
    "ds_read_b64_tr_b16 v[68:69], v7 offset:20480\n"
    "v_mfma_f32_16x16x32_bf16 a[124:127], v[188:191], a[148:151], a[124:127]\n"
    "ds_read_b64_tr_b16 v[70:71], v7 offset:20992\n"
    "v_mfma_f32_16x16x32_bf16 a[124:127], v[168:171], a[144:147], a[124:127]\n"
    "ds_read_b64_tr_b16 v[52:53], v8 offset:20480\n"
    "s_waitcnt lgkmcnt(6)\n"
    "v_mfma_f32_16x16x32_bf16 a[0:3], v[212:215], a[252:255], a[0:3]\n"
    "ds_read_b64_tr_b16 v[54:55], v8 offset:20992\n"
    "v_mfma_f32_16x16x32_bf16 a[0:3], v[60:63], a[128:131], a[0:3]\n"
    "s_waitcnt lgkmcnt(5)\n"
    "v_mfma_f32_16x16x32_bf16 a[0:3], v[48:51], a[132:135], a[0:3]\n"
    "v_mfma_f32_16x16x32_bf16 a[16:19], v[48:51], a[140:143], a[16:19]\n"
    "v_mfma_f32_16x16x32_bf16 a[16:19], v[60:63], a[132:135], a[16:19]\n"
    "v_mfma_f32_16x16x32_bf16 a[16:19], v[212:215], a[128:131], a[16:19]\n"
    "v_mfma_f32_16x16x32_bf16 a[32:35], v[212:215], a[132:135], a[32:35]\n"
    "v_mfma_f32_16x16x32_bf16 a[32:35], v[60:63], a[140:143], a[32:35]\n"
    "v_mfma_f32_16x16x32_bf16 a[32:35], v[48:51], a[144:147], a[32:35]\n"
    "v_mfma_f32_16x16x32_bf16 a[48:51], v[48:51], a[148:151], a[48:51]\n"
    "v_mfma_f32_16x16x32_bf16 a[48:51], v[60:63], a[144:147], a[48:51]\n"
    "v_mfma_f32_16x16x32_bf16 a[48:51], v[212:215], a[140:143], a[48:51]\n"
    "v_mfma_f32_16x16x32_bf16 a[64:67], v[212:215], a[144:147], a[64:67]\n"
    "v_mfma_f32_16x16x32_bf16 a[64:67], v[60:63], a[148:151], a[64:67]\n"
    "v_mfma_f32_16x16x32_bf16 a[64:67], v[48:51], a[156:159], a[64:67]\n"
    "v_mfma_f32_16x16x32_bf16 a[80:83], v[48:51], a[160:163], a[80:83]\n"
    "v_mfma_f32_16x16x32_bf16 a[80:83], v[60:63], a[156:159], a[80:83]\n"
    "v_mfma_f32_16x16x32_bf16 a[80:83], v[212:215], a[148:151], a[80:83]\n"
    "v_mfma_f32_16x16x32_bf16 a[96:99], v[212:215], a[156:159], a[96:99]\n"
    "v_mfma_f32_16x16x32_bf16 a[96:99], v[60:63], a[160:163], a[96:99]\n"
    "v_mfma_f32_16x16x32_bf16 a[96:99], v[48:51], a[164:167], a[96:99]\n"
    "v_mfma_f32_16x16x32_bf16 a[112:115], v[48:51], a[136:139], a[112:115]\n"
    "v_mfma_f32_16x16x32_bf16 a[112:115], v[60:63], a[164:167], a[112:115]\n"
    "v_mfma_f32_16x16x32_bf16 a[112:115], v[212:215], a[160:163], a[112:115]\n"
    "v_mfma_f32_16x16x32_bf16 a[4:7], v[196:199], a[252:255], a[4:7]\n"
    "v_mfma_f32_16x16x32_bf16 a[4:7], v[44:47], a[128:131], a[4:7]\n"
    "s_waitcnt lgkmcnt(0)\n"
    "v_mfma_f32_16x16x32_bf16 a[4:7], v[244:247], a[132:135], a[4:7]\n"
    "v_mfma_f32_16x16x32_bf16 a[20:23], v[244:247], a[140:143], a[20:23]\n"
    "v_mfma_f32_16x16x32_bf16 a[20:23], v[44:47], a[132:135], a[20:23]\n"
    "v_mfma_f32_16x16x32_bf16 a[20:23], v[196:199], a[128:131], a[20:23]\n"
    "v_mfma_f32_16x16x32_bf16 a[36:39], v[196:199], a[132:135], a[36:39]\n"
    "v_mfma_f32_16x16x32_bf16 a[36:39], v[44:47], a[140:143], a[36:39]\n"
    "v_mfma_f32_16x16x32_bf16 a[36:39], v[244:247], a[144:147], a[36:39]\n"
    "v_mfma_f32_16x16x32_bf16 a[52:55], v[244:247], a[148:151], a[52:55]\n"
    "v_mfma_f32_16x16x32_bf16 a[52:55], v[44:47], a[144:147], a[52:55]\n"
    "v_mfma_f32_16x16x32_bf16 a[52:55], v[196:199], a[140:143], a[52:55]\n"
    "v_mfma_f32_16x16x32_bf16 a[68:71], v[196:199], a[144:147], a[68:71]\n"
    "v_mfma_f32_16x16x32_bf16 a[68:71], v[44:47], a[148:151], a[68:71]\n"
    "v_mfma_f32_16x16x32_bf16 a[68:71], v[244:247], a[156:159], a[68:71]\n"
    "v_mfma_f32_16x16x32_bf16 a[84:87], v[244:247], a[160:163], a[84:87]\n"
    "v_mfma_f32_16x16x32_bf16 a[84:87], v[44:47], a[156:159], a[84:87]\n"
    "v_mfma_f32_16x16x32_bf16 a[84:87], v[196:199], a[148:151], a[84:87]\n"
    "v_mfma_f32_16x16x32_bf16 a[100:103], v[196:199], a[156:159], a[100:103]\n"
    "v_mfma_f32_16x16x32_bf16 a[100:103], v[44:47], a[160:163], a[100:103]\n"
    "v_mfma_f32_16x16x32_bf16 a[100:103], v[244:247], a[164:167], a[100:103]\n"
    "v_mfma_f32_16x16x32_bf16 a[116:119], v[244:247], a[136:139], a[116:119]\n"
    "v_mfma_f32_16x16x32_bf16 a[116:119], v[44:47], a[164:167], a[116:119]\n"
    "v_mfma_f32_16x16x32_bf16 a[116:119], v[196:199], a[160:163], a[116:119]\n"
    "v_mfma_f32_16x16x32_bf16 a[8:11], v[216:219], a[252:255], a[8:11]\n"
    "v_mfma_f32_16x16x32_bf16 a[8:11], v[240:243], a[128:131], a[8:11]\n"
    "v_mfma_f32_16x16x32_bf16 a[8:11], v[68:71], a[132:135], a[8:11]\n"
    "v_mfma_f32_16x16x32_bf16 a[24:27], v[68:71], a[140:143], a[24:27]\n"
    "v_mfma_f32_16x16x32_bf16 a[24:27], v[240:243], a[132:135], a[24:27]\n"
    "v_mfma_f32_16x16x32_bf16 a[24:27], v[216:219], a[128:131], a[24:27]\n"
    "v_mfma_f32_16x16x32_bf16 a[40:43], v[216:219], a[132:135], a[40:43]\n"
    "v_mfma_f32_16x16x32_bf16 a[40:43], v[240:243], a[140:143], a[40:43]\n"
    "v_mfma_f32_16x16x32_bf16 a[40:43], v[68:71], a[144:147], a[40:43]\n"
    "v_mfma_f32_16x16x32_bf16 a[56:59], v[68:71], a[148:151], a[56:59]\n"
    "v_mfma_f32_16x16x32_bf16 a[56:59], v[240:243], a[144:147], a[56:59]\n"
    "v_mfma_f32_16x16x32_bf16 a[56:59], v[216:219], a[140:143], a[56:59]\n"
    "s_mov_b32 m0, s70\n"
    "v_mfma_f32_16x16x32_bf16 a[72:75], v[216:219], a[144:147], a[72:75]\n"
    "v_add_u32 v3, 0x40000, v3\n"
    "v_add_u32 v4, 0x40000, v4\n"
    "v_mfma_f32_16x16x32_bf16 a[72:75], v[240:243], a[148:151], a[72:75]\n"
    "v_add_u32 v1, 0x40000, v1\n"
    "v_add_u32 v2, 0x40000, v2\n"
    "v_mfma_f32_16x16x32_bf16 a[72:75], v[68:71], a[156:159], a[72:75]\n"
    "buffer_load_dwordx4 v1, s[44:47], s52 offen offset:0 lds\n"
    "v_mfma_f32_16x16x32_bf16 a[88:91], v[68:71], a[160:163], a[88:91]\n"
    "v_mfma_f32_16x16x32_bf16 a[88:91], v[240:243], a[156:159], a[88:91]\n"
    "v_mfma_f32_16x16x32_bf16 a[88:91], v[216:219], a[148:151], a[88:91]\n"
    "v_mfma_f32_16x16x32_bf16 a[104:107], v[216:219], a[156:159], a[104:107]\n"
    "buffer_load_dwordx4 v2, s[44:47], s52 offen offset:1024 lds\n"
    "v_mfma_f32_16x16x32_bf16 a[104:107], v[240:243], a[160:163], a[104:107]\n"
    "v_mfma_f32_16x16x32_bf16 a[104:107], v[68:71], a[164:167], a[104:107]\n"
    "v_mfma_f32_16x16x32_bf16 a[120:123], v[68:71], a[136:139], a[120:123]\n"
    "v_mfma_f32_16x16x32_bf16 a[120:123], v[240:243], a[164:167], a[120:123]\n"
    "buffer_load_dwordx4 v3, s[44:47], s52 offen offset:2048 lds\n"
    "v_mfma_f32_16x16x32_bf16 a[120:123], v[216:219], a[160:163], a[120:123]\n"
    "s_waitcnt vmcnt(20)\n"
    "ds_read_b64_tr_b16 v[248:249], v5 offset:24576\n"
    "v_mfma_f32_16x16x32_bf16 a[12:15], v[56:59], a[252:255], a[12:15]\n"
    "ds_read_b64_tr_b16 v[250:251], v5 offset:25088\n"
    "v_mfma_f32_16x16x32_bf16 a[12:15], v[64:67], a[128:131], a[12:15]\n"
    "ds_read_b64_tr_b16 v[128:129], v6 offset:24576\n"
    "v_mfma_f32_16x16x32_bf16 a[12:15], v[52:55], a[132:135], a[12:15]\n"
    "ds_read_b64_tr_b16 v[130:131], v6 offset:25088\n"
    "buffer_load_dwordx4 v4, s[44:47], s52 offen offset:3072 lds\n"
    "v_mfma_f32_16x16x32_bf16 a[28:31], v[52:55], a[140:143], a[28:31]\n"
    "ds_read_b64_tr_b16 v[88:89], v7 offset:24576\n"
    "v_mfma_f32_16x16x32_bf16 a[28:31], v[64:67], a[132:135], a[28:31]\n"
    "ds_read_b64_tr_b16 v[90:91], v7 offset:25088\n"
    "v_mfma_f32_16x16x32_bf16 a[28:31], v[56:59], a[128:131], a[28:31]\n"
    "ds_read_b64_tr_b16 v[72:73], v8 offset:24576\n"
    "s_add_u32 m0, s75, 28160\n"
    "v_mfma_f32_16x16x32_bf16 a[44:47], v[56:59], a[132:135], a[44:47]\n"
    "ds_read_b64_tr_b16 v[74:75], v8 offset:25088\n"
    "buffer_load_dwordx4 v12, s[40:43], 0 offen offset:512 lds\n"
    "v_mfma_f32_16x16x32_bf16 a[44:47], v[64:67], a[140:143], a[44:47]\n"
    "v_mfma_f32_16x16x32_bf16 a[44:47], v[52:55], a[144:147], a[44:47]\n"
    "s_waitcnt vmcnt(18)\n"
    "ds_read_b64_tr_b16 v[112:113], v5 offset:0\n"
    "v_mfma_f32_16x16x32_bf16 a[60:63], v[52:55], a[148:151], a[60:63]\n"
    "ds_read_b64_tr_b16 v[114:115], v5 offset:512\n"
    "s_add_u32 m0, s75, 29184\n"
    "v_mfma_f32_16x16x32_bf16 a[60:63], v[64:67], a[144:147], a[60:63]\n"
    "ds_read_b64_tr_b16 v[124:125], v6 offset:0\n"
    "buffer_load_dwordx4 v13, s[40:43], 0 offen offset:512 lds\n"
    "v_mfma_f32_16x16x32_bf16 a[60:63], v[56:59], a[140:143], a[60:63]\n"
    "ds_read_b64_tr_b16 v[126:127], v6 offset:512\n"
    "v_mfma_f32_16x16x32_bf16 a[76:79], v[56:59], a[144:147], a[76:79]\n"
    "ds_read_b64_tr_b16 v[76:77], v7 offset:0\n"
    "v_mfma_f32_16x16x32_bf16 a[76:79], v[64:67], a[148:151], a[76:79]\n"
    "ds_read_b64_tr_b16 v[78:79], v7 offset:512\n"
    "s_add_u32 m0, s75, 30208\n"
    "v_mfma_f32_16x16x32_bf16 a[76:79], v[52:55], a[156:159], a[76:79]\n"
    "ds_read_b64_tr_b16 v[96:97], v8 offset:0\n"
    "buffer_load_dwordx4 v14, s[40:43], 0 offen offset:512 lds\n"
    "L_st3_%=:\n"
    "s_cbranch_execz L_back_%=\n"
    "v_mfma_f32_16x16x32_bf16 a[92:95], v[52:55], a[160:163], a[92:95]\n"
    "ds_read_b64_tr_b16 v[98:99], v8 offset:512\n"
    "v_mfma_f32_16x16x32_bf16 a[92:95], v[64:67], a[156:159], a[92:95]\n"
    "v_mfma_f32_16x16x32_bf16 a[92:95], v[56:59], a[148:151], a[92:95]\n"
    "s_waitcnt vmcnt(16)\n"
    "ds_read_b64_tr_b16 v[108:109], v5 offset:4096\n"
    "s_add_u32 m0, s75, 31232\n"
    "v_mfma_f32_16x16x32_bf16 a[108:111], v[56:59], a[156:159], a[108:111]\n"
    "ds_read_b64_tr_b16 v[110:111], v5 offset:4608\n"
    "buffer_load_dwordx4 v15, s[40:43], 0 offen offset:512 lds\n"
    "v_mfma_f32_16x16x32_bf16 a[108:111], v[64:67], a[160:163], a[108:111]\n"
    "ds_read_b64_tr_b16 v[80:81], v6 offset:4096\n"
    "v_mfma_f32_16x16x32_bf16 a[108:111], v[52:55], a[164:167], a[108:111]\n"
    "ds_read_b64_tr_b16 v[82:83], v6 offset:4608\n"
    "v_mfma_f32_16x16x32_bf16 a[124:127], v[52:55], a[136:139], a[124:127]\n"
    "ds_read_b64_tr_b16 v[132:133], v7 offset:4096\n"
    "s_mov_b32 m0, s71\n"
    "v_mfma_f32_16x16x32_bf16 a[124:127], v[64:67], a[164:167], a[124:127]\n"
    "ds_read_b64_tr_b16 v[134:135], v7 offset:4608\n"
    "buffer_load_dwordx4 v1, s[44:47], s53 offen offset:0 lds\n"
    "v_mfma_f32_16x16x32_bf16 a[124:127], v[56:59], a[160:163], a[124:127]\n"
    "ds_read_b64_tr_b16 v[92:93], v8 offset:4096\n"
    "s_waitcnt lgkmcnt(6)\n"
    "v_mfma_f32_16x16x32_bf16 a[0:3], v[248:251], a[140:143], a[0:3]\n"
    "ds_read_b64_tr_b16 v[94:95], v8 offset:4608\n"
    "v_mfma_f32_16x16x32_bf16 a[0:3], v[112:115], a[144:147], a[0:3]\n"
    "s_waitcnt lgkmcnt(5)\n"
    "v_mfma_f32_16x16x32_bf16 a[0:3], v[108:111], a[148:151], a[0:3]\n"
    "buffer_load_dwordx4 v2, s[44:47], s53 offen offset:1024 lds\n"
    "v_mfma_f32_16x16x32_bf16 a[16:19], v[108:111], a[156:159], a[16:19]\n"
    "v_mfma_f32_16x16x32_bf16 a[16:19], v[112:115], a[148:151], a[16:19]\n"
    "v_mfma_f32_16x16x32_bf16 a[16:19], v[248:251], a[144:147], a[16:19]\n"
    "v_mfma_f32_16x16x32_bf16 a[32:35], v[248:251], a[148:151], a[32:35]\n"
    "buffer_load_dwordx4 v3, s[44:47], s53 offen offset:2048 lds\n"
    "v_mfma_f32_16x16x32_bf16 a[32:35], v[112:115], a[156:159], a[32:35]\n"
    "v_mfma_f32_16x16x32_bf16 a[32:35], v[108:111], a[160:163], a[32:35]\n"
    "v_mfma_f32_16x16x32_bf16 a[48:51], v[108:111], a[164:167], a[48:51]\n"
    "v_mfma_f32_16x16x32_bf16 a[48:51], v[112:115], a[160:163], a[48:51]\n"
    "buffer_load_dwordx4 v4, s[44:47], s53 offen offset:3072 lds\n"
    "v_mfma_f32_16x16x32_bf16 a[48:51], v[248:251], a[156:159], a[48:51]\n"
    "v_mfma_f32_16x16x32_bf16 a[64:67], v[248:251], a[160:163], a[64:67]\n"
    "v_mfma_f32_16x16x32_bf16 a[64:67], v[112:115], a[164:167], a[64:67]\n"
    "s_add_u32 m0, s75, 32256\n"
    "v_mfma_f32_16x16x32_bf16 a[64:67], v[108:111], a[136:139], a[64:67]\n"
    "buffer_load_dwordx4 v16, s[40:43], 0 offen offset:512 lds\n"
    "v_mfma_f32_16x16x32_bf16 a[80:83], v[108:111], a[152:155], a[80:83]\n"
    "v_mfma_f32_16x16x32_bf16 a[80:83], v[112:115], a[136:139], a[80:83]\n"
    "v_mfma_f32_16x16x32_bf16 a[80:83], v[248:251], a[164:167], a[80:83]\n"
    "s_add_u32 m0, s75, 33280\n"
    "v_mfma_f32_16x16x32_bf16 a[96:99], v[248:251], a[136:139], a[96:99]\n"
    "buffer_load_dwordx4 v17, s[40:43], 0 offen offset:512 lds\n"
    "v_mfma_f32_16x16x32_bf16 a[96:99], v[112:115], a[152:155], a[96:99]\n"
    "v_mfma_f32_16x16x32_bf16 a[96:99], v[108:111], a[168:171], a[96:99]\n"
    "v_mfma_f32_16x16x32_bf16 a[112:115], v[108:111], a[172:175], a[112:115]\n"
    "s_add_u32 m0, s75, 34304\n"
    "v_mfma_f32_16x16x32_bf16 a[112:115], v[112:115], a[168:171], a[112:115]\n"
    "buffer_load_dwordx4 v18, s[40:43], 0 offen offset:512 lds\n"
    "v_mfma_f32_16x16x32_bf16 a[112:115], v[248:251], a[152:155], a[112:115]\n"
    "v_mfma_f32_16x16x32_bf16 a[4:7], v[128:131], a[140:143], a[4:7]\n"
    "v_mfma_f32_16x16x32_bf16 a[4:7], v[124:127], a[144:147], a[4:7]\n"
    "s_add_u32 m0, s75, 35328\n"
    "s_waitcnt lgkmcnt(0)\n"
    "v_mfma_f32_16x16x32_bf16 a[4:7], v[80:83], a[148:151], a[4:7]\n"
    "buffer_load_dwordx4 v19, s[40:43], 0 offen offset:512 lds\n"
    "v_mfma_f32_16x16x32_bf16 a[20:23], v[80:83], a[156:159], a[20:23]\n"
    "v_mfma_f32_16x16x32_bf16 a[20:23], v[124:127], a[148:151], a[20:23]\n"
    "v_mfma_f32_16x16x32_bf16 a[20:23], v[128:131], a[144:147], a[20:23]\n"
    "s_mov_b32 m0, s72\n"
    "v_mfma_f32_16x16x32_bf16 a[36:39], v[128:131], a[148:151], a[36:39]\n"
    "buffer_load_dwordx4 v1, s[44:47], s54 offen offset:0 lds\n"
    "v_mfma_f32_16x16x32_bf16 a[36:39], v[124:127], a[156:159], a[36:39]\n"
    "v_mfma_f32_16x16x32_bf16 a[36:39], v[80:83], a[160:163], a[36:39]\n"
    "v_mfma_f32_16x16x32_bf16 a[52:55], v[80:83], a[164:167], a[52:55]\n"
    "v_mfma_f32_16x16x32_bf16 a[52:55], v[124:127], a[160:163], a[52:55]\n"
    "buffer_load_dwordx4 v2, s[44:47], s54 offen offset:1024 lds\n"
    "v_mfma_f32_16x16x32_bf16 a[52:55], v[128:131], a[156:159], a[52:55]\n"
    "v_mfma_f32_16x16x32_bf16 a[68:71], v[128:131], a[160:163], a[68:71]\n"
    "v_mfma_f32_16x16x32_bf16 a[68:71], v[124:127], a[164:167], a[68:71]\n"
    "v_mfma_f32_16x16x32_bf16 a[68:71], v[80:83], a[136:139], a[68:71]\n"
    "buffer_load_dwordx4 v3, s[44:47], s54 offen offset:2048 lds\n"
    "v_mfma_f32_16x16x32_bf16 a[84:87], v[80:83], a[152:155], a[84:87]\n"
    "v_mfma_f32_16x16x32_bf16 a[84:87], v[124:127], a[136:139], a[84:87]\n"
    "v_mfma_f32_16x16x32_bf16 a[84:87], v[128:131], a[164:167], a[84:87]\n"
    "v_mfma_f32_16x16x32_bf16 a[100:103], v[128:131], a[136:139], a[100:103]\n"
    "buffer_load_dwordx4 v4, s[44:47], s54 offen offset:3072 lds\n"
    "v_mfma_f32_16x16x32_bf16 a[100:103], v[124:127], a[152:155], a[100:103]\n"
    "v_mfma_f32_16x16x32_bf16 a[100:103], v[80:83], a[168:171], a[100:103]\n"
    "v_mfma_f32_16x16x32_bf16 a[116:119], v[80:83], a[172:175], a[116:119]\n"
    "s_add_u32 m0, s75, 36352\n"
    "v_mfma_f32_16x16x32_bf16 a[116:119], v[124:127], a[168:171], a[116:119]\n"
    "buffer_load_dwordx4 v20, s[40:43], 0 offen offset:512 lds\n"
    "v_mfma_f32_16x16x32_bf16 a[116:119], v[128:131], a[152:155], a[116:119]\n"
    "v_mfma_f32_16x16x32_bf16 a[8:11], v[88:91], a[140:143], a[8:11]\n"
    "v_mfma_f32_16x16x32_bf16 a[8:11], v[76:79], a[144:147], a[8:11]\n"
    "s_add_u32 m0, s75, 37376\n"
    "v_mfma_f32_16x16x32_bf16 a[8:11], v[132:135], a[148:151], a[8:11]\n"
    "buffer_load_dwordx4 v21, s[40:43], 0 offen offset:512 lds\n"
    "v_mfma_f32_16x16x32_bf16 a[24:27], v[132:135], a[156:159], a[24:27]\n"
    "v_mfma_f32_16x16x32_bf16 a[24:27], v[76:79], a[148:151], a[24:27]\n"
    "v_mfma_f32_16x16x32_bf16 a[24:27], v[88:91], a[144:147], a[24:27]\n"
    "s_mov_b32 m0, s73\n"
    "v_mfma_f32_16x16x32_bf16 a[40:43], v[88:91], a[148:151], a[40:43]\n"
    "buffer_load_dwordx4 v1, s[44:47], s55 offen offset:0 lds\n"
    "v_mfma_f32_16x16x32_bf16 a[40:43], v[76:79], a[156:159], a[40:43]\n"
    "v_mfma_f32_16x16x32_bf16 a[40:43], v[132:135], a[160:163], a[40:43]\n"
    "v_mfma_f32_16x16x32_bf16 a[56:59], v[132:135], a[164:167], a[56:59]\n"
    "v_mfma_f32_16x16x32_bf16 a[56:59], v[76:79], a[160:163], a[56:59]\n"
    "buffer_load_dwordx4 v2, s[44:47], s55 offen offset:1024 lds\n"
    "v_mfma_f32_16x16x32_bf16 a[56:59], v[88:91], a[156:159], a[56:59]\n"
    "v_mfma_f32_16x16x32_bf16 a[72:75], v[88:91], a[160:163], a[72:75]\n"
    "v_mfma_f32_16x16x32_bf16 a[72:75], v[76:79], a[164:167], a[72:75]\n"
    "v_mfma_f32_16x16x32_bf16 a[72:75], v[132:135], a[136:139], a[72:75]\n"
    "buffer_load_dwordx4 v3, s[44:47], s55 offen offset:2048 lds\n"
    "v_mfma_f32_16x16x32_bf16 a[88:91], v[132:135], a[152:155], a[88:91]\n"
    "v_mfma_f32_16x16x32_bf16 a[88:91], v[76:79], a[136:139], a[88:91]\n"
    "v_mfma_f32_16x16x32_bf16 a[88:91], v[88:91], a[164:167], a[88:91]\n"
    "v_mfma_f32_16x16x32_bf16 a[104:107], v[88:91], a[136:139], a[104:107]\n"
    "buffer_load_dwordx4 v4, s[44:47], s55 offen offset:3072 lds\n"
    "v_mfma_f32_16x16x32_bf16 a[104:107], v[76:79], a[152:155], a[104:107]\n"
    "v_mfma_f32_16x16x32_bf16 a[104:107], v[132:135], a[168:171], a[104:107]\n"
    "v_mfma_f32_16x16x32_bf16 a[120:123], v[132:135], a[172:175], a[120:123]\n"
    "s_mov_b32 m0, s74\n"
    "v_mfma_f32_16x16x32_bf16 a[120:123], v[76:79], a[168:171], a[120:123]\n"
    "buffer_load_dwordx4 v1, s[44:47], s56 offen offset:0 lds\n"
    "v_mfma_f32_16x16x32_bf16 a[120:123], v[88:91], a[152:155], a[120:123]\n"
    "s_waitcnt vmcnt(23)\n"
    "ds_read_b64_tr_b16 v[84:85], v5 offset:8192\n"
    "v_mfma_f32_16x16x32_bf16 a[12:15], v[72:75], a[140:143], a[12:15]\n"
    "ds_read_b64_tr_b16 v[86:87], v5 offset:8704\n"
    "v_mfma_f32_16x16x32_bf16 a[12:15], v[96:99], a[144:147], a[12:15]\n"
    "ds_read_b64_tr_b16 v[176:177], v6 offset:8192\n"
    "v_mfma_f32_16x16x32_bf16 a[12:15], v[92:95], a[148:151], a[12:15]\n"
    "ds_read_b64_tr_b16 v[178:179], v6 offset:8704\n"
    "buffer_load_dwordx4 v2, s[44:47], s56 offen offset:1024 lds\n"
    "v_mfma_f32_16x16x32_bf16 a[28:31], v[92:95], a[156:159], a[28:31]\n"
    "ds_read_b64_tr_b16 v[104:105], v7 offset:8192\n"
    "v_mfma_f32_16x16x32_bf16 a[28:31], v[96:99], a[148:151], a[28:31]\n"
    "ds_read_b64_tr_b16 v[106:107], v7 offset:8704\n"
    "v_mfma_f32_16x16x32_bf16 a[28:31], v[72:75], a[144:147], a[28:31]\n"
    "ds_read_b64_tr_b16 v[116:117], v8 offset:8192\n"
    "v_mfma_f32_16x16x32_bf16 a[44:47], v[72:75], a[148:151], a[44:47]\n"
    "ds_read_b64_tr_b16 v[118:119], v8 offset:8704\n"
    "buffer_load_dwordx4 v3, s[44:47], s56 offen offset:2048 lds\n"
    "v_mfma_f32_16x16x32_bf16 a[44:47], v[96:99], a[156:159], a[44:47]\n"
    "v_mfma_f32_16x16x32_bf16 a[44:47], v[92:95], a[160:163], a[44:47]\n"
    "s_waitcnt vmcnt(17)\n"
    "ds_read_b64_tr_b16 v[160:161], v5 offset:12288\n"
    "v_mfma_f32_16x16x32_bf16 a[60:63], v[92:95], a[164:167], a[60:63]\n"
    "ds_read_b64_tr_b16 v[162:163], v5 offset:12800\n"
    "v_mfma_f32_16x16x32_bf16 a[60:63], v[96:99], a[160:163], a[60:63]\n"
    "ds_read_b64_tr_b16 v[172:173], v6 offset:12288\n"
    "buffer_load_dwordx4 v4, s[44:47], s56 offen offset:3072 lds\n"
    "v_mfma_f32_16x16x32_bf16 a[60:63], v[72:75], a[156:159], a[60:63]\n"
    "ds_read_b64_tr_b16 v[174:175], v6 offset:12800\n"
    "v_mfma_f32_16x16x32_bf16 a[76:79], v[72:75], a[160:163], a[76:79]\n"
    "ds_read_b64_tr_b16 v[100:101], v7 offset:12288\n"
    "v_mfma_f32_16x16x32_bf16 a[76:79], v[96:99], a[164:167], a[76:79]\n"
    "ds_read_b64_tr_b16 v[102:103], v7 offset:12800\n"
    "s_mov_b32 m0, s68\n"
    "v_mfma_f32_16x16x32_bf16 a[76:79], v[92:95], a[136:139], a[76:79]\n"
    "ds_read_b64_tr_b16 v[144:145], v8 offset:12288\n"
    "buffer_load_dwordx4 v1, s[44:47], s57 offen offset:0 lds\n"
    "v_mfma_f32_16x16x32_bf16 a[92:95], v[92:95], a[152:155], a[92:95]\n"
    "ds_read_b64_tr_b16 v[146:147], v8 offset:12800\n"
    "v_mfma_f32_16x16x32_bf16 a[92:95], v[96:99], a[136:139], a[92:95]\n"
    "ds_read_b128 a[176:179], v9 offset:28672\n"
    "v_mfma_f32_16x16x32_bf16 a[92:95], v[72:75], a[164:167], a[92:95]\n"
    "s_waitcnt vmcnt(11)\n"
    "ds_read_b64_tr_b16 v[156:157], v5 offset:16384\n"
    "ds_read_b128 a[180:183], v9 offset:29952\n"
    "v_mfma_f32_16x16x32_bf16 a[108:111], v[72:75], a[136:139], a[108:111]\n"
    "ds_read_b64_tr_b16 v[158:159], v5 offset:16896\n"
    "ds_read_b128 a[184:187], v9 offset:31232\n"
    "v_mfma_f32_16x16x32_bf16 a[108:111], v[96:99], a[152:155], a[108:111]\n"
    "ds_read_b64_tr_b16 v[136:137], v6 offset:16384\n"
    "ds_read_b128 a[188:191], v9 offset:32512\n"
    "v_mfma_f32_16x16x32_bf16 a[108:111], v[92:95], a[168:171], a[108:111]\n"
    "ds_read_b64_tr_b16 v[138:139], v6 offset:16896\n"
    "buffer_load_dwordx4 v2, s[44:47], s57 offen offset:1024 lds\n"
    "v_mfma_f32_16x16x32_bf16 a[124:127], v[92:95], a[172:175], a[124:127]\n"
    "ds_read_b64_tr_b16 v[180:181], v7 offset:16384\n"
    "v_mfma_f32_16x16x32_bf16 a[124:127], v[96:99], a[168:171], a[124:127]\n"
    "ds_read_b64_tr_b16 v[182:183], v7 offset:16896\n"
    "v_mfma_f32_16x16x32_bf16 a[124:127], v[72:75], a[152:155], a[124:127]\n"
    "ds_read_b64_tr_b16 v[140:141], v8 offset:16384\n"
    "s_waitcnt lgkmcnt(8)\n"
    "v_mfma_f32_16x16x32_bf16 a[0:3], v[84:87], a[176:179], a[0:3]\n"
    "ds_read_b64_tr_b16 v[142:143], v8 offset:16896\n"
    "ds_read_b128 a[192:195], v9 offset:33792\n"
    "v_mfma_f32_16x16x32_bf16 a[0:3], v[160:163], a[180:183], a[0:3]\n"
    "ds_read_b128 a[196:199], v9 offset:35072\n"
    "buffer_load_dwordx4 v3, s[44:47], s57 offen offset:2048 lds\n"
    "s_waitcnt lgkmcnt(7)\n"
    "v_mfma_f32_16x16x32_bf16 a[0:3], v[156:159], a[184:187], a[0:3]\n"
    "v_mfma_f32_16x16x32_bf16 a[16:19], v[156:159], a[188:191], a[16:19]\n"
    "v_mfma_f32_16x16x32_bf16 a[16:19], v[160:163], a[184:187], a[16:19]\n"
    "v_mfma_f32_16x16x32_bf16 a[16:19], v[84:87], a[180:183], a[16:19]\n"
    "buffer_load_dwordx4 v4, s[44:47], s57 offen offset:3072 lds\n"
    "v_mfma_f32_16x16x32_bf16 a[32:35], v[84:87], a[184:187], a[32:35]\n"
    "s_waitcnt vmcnt(13)\n"
    "ds_read_b128 a[200:203], v9 offset:36352\n"
    "v_mfma_f32_16x16x32_bf16 a[32:35], v[160:163], a[188:191], a[32:35]\n"
    "s_waitcnt vmcnt(12)\n"
    "ds_read_b128 a[212:215], v9 offset:37632\n"
    "s_waitcnt lgkmcnt(2)\n"
    "v_mfma_f32_16x16x32_bf16 a[32:35], v[156:159], a[192:195], a[32:35]\n"
    "s_mov_b32 m0, s69\n"
    "v_mfma_f32_16x16x32_bf16 a[48:51], v[156:159], a[196:199], a[48:51]\n"
    "buffer_load_dwordx4 v1, s[44:47], s58 offen offset:0 lds\n"
    "v_mfma_f32_16x16x32_bf16 a[48:51], v[160:163], a[192:195], a[48:51]\n"
    "v_mfma_f32_16x16x32_bf16 a[48:51], v[84:87], a[188:191], a[48:51]\n"
    "v_mfma_f32_16x16x32_bf16 a[64:67], v[84:87], a[192:195], a[64:67]\n"
    "ds_read_b128 a[208:211], v10 offset:28672\n"
    "v_mfma_f32_16x16x32_bf16 a[64:67], v[160:163], a[196:199], a[64:67]\n"
    "ds_read_b128 a[204:207], v10 offset:29952\n"
    "buffer_load_dwordx4 v2, s[44:47], s58 offen offset:1024 lds\n"
    "s_waitcnt lgkmcnt(2)\n"
    "v_mfma_f32_16x16x32_bf16 a[64:67], v[156:159], a[200:203], a[64:67]\n"
    "v_mfma_f32_16x16x32_bf16 a[80:83], v[156:159], a[212:215], a[80:83]\n"
    "v_mfma_f32_16x16x32_bf16 a[80:83], v[160:163], a[200:203], a[80:83]\n"
    "v_mfma_f32_16x16x32_bf16 a[80:83], v[84:87], a[196:199], a[80:83]\n"
    "buffer_load_dwordx4 v3, s[44:47], s58 offen offset:2048 lds\n"
    "v_mfma_f32_16x16x32_bf16 a[96:99], v[84:87], a[200:203], a[96:99]\n"
    "v_mfma_f32_16x16x32_bf16 a[96:99], v[160:163], a[212:215], a[96:99]\n"
    "s_waitcnt lgkmcnt(0)\n"
    "v_mfma_f32_16x16x32_bf16 a[96:99], v[156:159], a[208:211], a[96:99]\n"
    "ds_read_b128 a[216:219], v10 offset:31232\n"
    "v_mfma_f32_16x16x32_bf16 a[112:115], v[156:159], a[204:207], a[112:115]\n"
    "buffer_load_dwordx4 v4, s[44:47], s58 offen offset:3072 lds\n"
    "v_mfma_f32_16x16x32_bf16 a[112:115], v[160:163], a[208:211], a[112:115]\n"
    "v_mfma_f32_16x16x32_bf16 a[112:115], v[84:87], a[212:215], a[112:115]\n"
    "ds_read_b128 a[220:223], v10 offset:32512\n"
    "v_mfma_f32_16x16x32_bf16 a[4:7], v[176:179], a[176:179], a[4:7]\n"
    "s_mov_b32 m0, s70\n"
    "v_mfma_f32_16x16x32_bf16 a[4:7], v[172:175], a[180:183], a[4:7]\n"
    "buffer_load_dwordx4 v1, s[44:47], s59 offen offset:0 lds\n"
    "v_mfma_f32_16x16x32_bf16 a[4:7], v[136:139], a[184:187], a[4:7]\n"
    "ds_read_b128 a[224:227], v10 offset:33792\n"
    "v_mfma_f32_16x16x32_bf16 a[20:23], v[136:139], a[188:191], a[20:23]\n"
    "v_mfma_f32_16x16x32_bf16 a[20:23], v[172:175], a[184:187], a[20:23]\n"
    "v_mfma_f32_16x16x32_bf16 a[20:23], v[176:179], a[180:183], a[20:23]\n"
    "ds_read_b128 a[228:231], v10 offset:35072\n"
    "buffer_load_dwordx4 v2, s[44:47], s59 offen offset:1024 lds\n"
    "v_mfma_f32_16x16x32_bf16 a[36:39], v[176:179], a[184:187], a[36:39]\n"
    "v_mfma_f32_16x16x32_bf16 a[36:39], v[172:175], a[188:191], a[36:39]\n"
    "v_mfma_f32_16x16x32_bf16 a[36:39], v[136:139], a[192:195], a[36:39]\n"
    "ds_read_b128 a[232:235], v10 offset:36352\n"
    "v_mfma_f32_16x16x32_bf16 a[52:55], v[136:139], a[196:199], a[52:55]\n"
    "buffer_load_dwordx4 v3, s[44:47], s59 offen offset:2048 lds\n"
    "v_mfma_f32_16x16x32_bf16 a[52:55], v[172:175], a[192:195], a[52:55]\n"
    "v_mfma_f32_16x16x32_bf16 a[52:55], v[176:179], a[188:191], a[52:55]\n"
    "ds_read_b128 a[236:239], v10 offset:37632\n"
    "v_mfma_f32_16x16x32_bf16 a[68:71], v[176:179], a[192:195], a[68:71]\n"
    "v_mfma_f32_16x16x32_bf16 a[68:71], v[172:175], a[196:199], a[68:71]\n"
    "buffer_load_dwordx4 v4, s[44:47], s59 offen offset:3072 lds\n"
    "v_mfma_f32_16x16x32_bf16 a[68:71], v[136:139], a[200:203], a[68:71]\n"
    "ds_read_b128 a[240:243], v11 offset:28928\n"
    "v_mfma_f32_16x16x32_bf16 a[84:87], v[136:139], a[212:215], a[84:87]\n"
    "v_mfma_f32_16x16x32_bf16 a[84:87], v[172:175], a[200:203], a[84:87]\n"
    "s_mov_b32 m0, s71\n"
    "v_mfma_f32_16x16x32_bf16 a[84:87], v[176:179], a[196:199], a[84:87]\n"
    "ds_read_b128 a[244:247], v11 offset:30208\n"
    "buffer_load_dwordx4 v1, s[44:47], s60 offen offset:0 lds\n"
    "v_mfma_f32_16x16x32_bf16 a[100:103], v[176:179], a[200:203], a[100:103]\n"
    "v_mfma_f32_16x16x32_bf16 a[100:103], v[172:175], a[212:215], a[100:103]\n"
    "v_mfma_f32_16x16x32_bf16 a[100:103], v[136:139], a[208:211], a[100:103]\n"
    "ds_read_b128 a[248:251], v11 offset:31488\n"
    "v_mfma_f32_16x16x32_bf16 a[116:119], v[136:139], a[204:207], a[116:119]\n"
    "buffer_load_dwordx4 v2, s[44:47], s60 offen offset:1024 lds\n"
    "v_mfma_f32_16x16x32_bf16 a[116:119], v[172:175], a[208:211], a[116:119]\n"
    "v_mfma_f32_16x16x32_bf16 a[116:119], v[176:179], a[212:215], a[116:119]\n"
    "ds_read_b128 a[252:255], v11 offset:32768\n"
    "v_mfma_f32_16x16x32_bf16 a[8:11], v[104:107], a[176:179], a[8:11]\n"
    "v_mfma_f32_16x16x32_bf16 a[8:11], v[100:103], a[180:183], a[8:11]\n"
    "buffer_load_dwordx4 v3, s[44:47], s60 offen offset:2048 lds\n"
    "v_mfma_f32_16x16x32_bf16 a[8:11], v[180:183], a[184:187], a[8:11]\n"
    "ds_read_b128 a[128:131], v11 offset:34048\n"
    "v_mfma_f32_16x16x32_bf16 a[24:27], v[180:183], a[188:191], a[24:27]\n"
    "v_mfma_f32_16x16x32_bf16 a[24:27], v[100:103], a[184:187], a[24:27]\n"
    "v_mfma_f32_16x16x32_bf16 a[24:27], v[104:107], a[180:183], a[24:27]\n"
    "s_waitcnt vmcnt(19)\n"
    "ds_read_b64_tr_b16 v[120:121], v5 offset:20480\n"
    "ds_read_b128 a[132:135], v11 offset:35328\n"
    "v_mfma_f32_16x16x32_bf16 a[40:43], v[104:107], a[184:187], a[40:43]\n"
    "ds_read_b64_tr_b16 v[122:123], v5 offset:20992\n"
    "buffer_load_dwordx4 v4, s[44:47], s60 offen offset:3072 lds\n"
    "v_mfma_f32_16x16x32_bf16 a[40:43], v[100:103], a[188:191], a[40:43]\n"
    "ds_read_b64_tr_b16 v[224:225], v6 offset:20480\n"
    "v_mfma_f32_16x16x32_bf16 a[40:43], v[180:183], a[192:195], a[40:43]\n"
    "ds_read_b64_tr_b16 v[226:227], v6 offset:20992\n"
    "v_mfma_f32_16x16x32_bf16 a[56:59], v[180:183], a[196:199], a[56:59]\n"
    "ds_read_b64_tr_b16 v[152:153], v7 offset:20480\n"
    "s_mov_b32 m0, s72\n"
    "v_mfma_f32_16x16x32_bf16 a[56:59], v[100:103], a[192:195], a[56:59]\n"
    "ds_read_b64_tr_b16 v[154:155], v7 offset:20992\n"
    "buffer_load_dwordx4 v1, s[44:47], s61 offen offset:0 lds\n"
    "v_mfma_f32_16x16x32_bf16 a[56:59], v[104:107], a[188:191], a[56:59]\n"
    "ds_read_b64_tr_b16 v[164:165], v8 offset:20480\n"
    "v_mfma_f32_16x16x32_bf16 a[72:75], v[104:107], a[192:195], a[72:75]\n"
    "ds_read_b64_tr_b16 v[166:167], v8 offset:20992\n"
    "v_mfma_f32_16x16x32_bf16 a[72:75], v[100:103], a[196:199], a[72:75]\n"
    "v_mfma_f32_16x16x32_bf16 a[72:75], v[180:183], a[200:203], a[72:75]\n"
    "buffer_load_dwordx4 v2, s[44:47], s61 offen offset:1024 lds\n"
    "v_mfma_f32_16x16x32_bf16 a[88:91], v[180:183], a[212:215], a[88:91]\n"
    "v_mfma_f32_16x16x32_bf16 a[88:91], v[100:103], a[200:203], a[88:91]\n"
    "v_mfma_f32_16x16x32_bf16 a[88:91], v[104:107], a[196:199], a[88:91]\n"
    "v_mfma_f32_16x16x32_bf16 a[104:107], v[104:107], a[200:203], a[104:107]\n"
    "buffer_load_dwordx4 v3, s[44:47], s61 offen offset:2048 lds\n"
    "v_mfma_f32_16x16x32_bf16 a[104:107], v[100:103], a[212:215], a[104:107]\n"
    "v_mfma_f32_16x16x32_bf16 a[104:107], v[180:183], a[208:211], a[104:107]\n"
    "v_mfma_f32_16x16x32_bf16 a[120:123], v[180:183], a[204:207], a[120:123]\n"
    "s_waitcnt vmcnt(19)\n"
    "ds_read_b64_tr_b16 v[208:209], v5 offset:24576\n"
    "v_mfma_f32_16x16x32_bf16 a[120:123], v[100:103], a[208:211], a[120:123]\n"
    "ds_read_b64_tr_b16 v[210:211], v5 offset:25088\n"
    "buffer_load_dwordx4 v4, s[44:47], s61 offen offset:3072 lds\n"
    "v_mfma_f32_16x16x32_bf16 a[120:123], v[104:107], a[212:215], a[120:123]\n"
    "ds_read_b64_tr_b16 v[220:221], v6 offset:24576\n"
    "v_mfma_f32_16x16x32_bf16 a[12:15], v[116:119], a[176:179], a[12:15]\n"
    "ds_read_b64_tr_b16 v[222:223], v6 offset:25088\n"
    "v_mfma_f32_16x16x32_bf16 a[12:15], v[144:147], a[180:183], a[12:15]\n"
    "ds_read_b64_tr_b16 v[148:149], v7 offset:24576\n"
    "s_mov_b32 m0, s73\n"
    "v_mfma_f32_16x16x32_bf16 a[12:15], v[140:143], a[184:187], a[12:15]\n"
    "ds_read_b64_tr_b16 v[150:151], v7 offset:25088\n"
    "s_waitcnt lgkmcnt(6)\n"
    "buffer_load_dwordx4 v1, s[44:47], s62 offen offset:0 lds\n"
    "v_mfma_f32_16x16x32_bf16 a[28:31], v[140:143], a[188:191], a[28:31]\n"
    "ds_read_b64_tr_b16 v[192:193], v8 offset:24576\n"
    "v_mfma_f32_16x16x32_bf16 a[28:31], v[144:147], a[184:187], a[28:31]\n"
    "ds_read_b64_tr_b16 v[194:195], v8 offset:25088\n"
    "v_mfma_f32_16x16x32_bf16 a[28:31], v[116:119], a[180:183], a[28:31]\n"
    "v_mfma_f32_16x16x32_bf16 a[44:47], v[116:119], a[184:187], a[44:47]\n"
    "buffer_load_dwordx4 v2, s[44:47], s62 offen offset:1024 lds\n"
    "v_mfma_f32_16x16x32_bf16 a[44:47], v[144:147], a[188:191], a[44:47]\n"
    "v_mfma_f32_16x16x32_bf16 a[44:47], v[140:143], a[192:195], a[44:47]\n"
    "v_mfma_f32_16x16x32_bf16 a[60:63], v[140:143], a[196:199], a[60:63]\n"
    "v_mfma_f32_16x16x32_bf16 a[60:63], v[144:147], a[192:195], a[60:63]\n"
    "buffer_load_dwordx4 v3, s[44:47], s62 offen offset:2048 lds\n"
    "v_mfma_f32_16x16x32_bf16 a[60:63], v[116:119], a[188:191], a[60:63]\n"
    "v_mfma_f32_16x16x32_bf16 a[76:79], v[116:119], a[192:195], a[76:79]\n"
    "v_mfma_f32_16x16x32_bf16 a[76:79], v[144:147], a[196:199], a[76:79]\n"
    "s_waitcnt vmcnt(19)\n"
    "ds_read_b64_tr_b16 v[204:205], v5 offset:0\n"
    "v_mfma_f32_16x16x32_bf16 a[76:79], v[140:143], a[200:203], a[76:79]\n"
    "ds_read_b64_tr_b16 v[206:207], v5 offset:512\n"
    "buffer_load_dwordx4 v4, s[44:47], s62 offen offset:3072 lds\n"
    "v_mfma_f32_16x16x32_bf16 a[92:95], v[140:143], a[212:215], a[92:95]\n"
    "ds_read_b64_tr_b16 v[184:185], v6 offset:0\n"
    "v_mfma_f32_16x16x32_bf16 a[92:95], v[144:147], a[200:203], a[92:95]\n"
    "ds_read_b64_tr_b16 v[186:187], v6 offset:512\n"
    "v_mfma_f32_16x16x32_bf16 a[92:95], v[116:119], a[196:199], a[92:95]\n"
    "ds_read_b64_tr_b16 v[228:229], v7 offset:0\n"
    "s_mov_b32 m0, s74\n"
    "v_mfma_f32_16x16x32_bf16 a[108:111], v[116:119], a[200:203], a[108:111]\n"
    "ds_read_b64_tr_b16 v[230:231], v7 offset:512\n"
    "s_waitcnt lgkmcnt(6)\n"
    "buffer_load_dwordx4 v1, s[44:47], s63 offen offset:0 lds\n"
    "v_mfma_f32_16x16x32_bf16 a[108:111], v[144:147], a[212:215], a[108:111]\n"
    "ds_read_b64_tr_b16 v[188:189], v8 offset:0\n"
    "v_mfma_f32_16x16x32_bf16 a[108:111], v[140:143], a[208:211], a[108:111]\n"
    "ds_read_b64_tr_b16 v[190:191], v8 offset:512\n"
    "v_mfma_f32_16x16x32_bf16 a[124:127], v[140:143], a[204:207], a[124:127]\n"
    "v_mfma_f32_16x16x32_bf16 a[124:127], v[144:147], a[208:211], a[124:127]\n"
    "buffer_load_dwordx4 v2, s[44:47], s63 offen offset:1024 lds\n"
    "v_mfma_f32_16x16x32_bf16 a[124:127], v[116:119], a[212:215], a[124:127]\n"
    "v_mfma_f32_16x16x32_bf16 a[0:3], v[120:123], a[188:191], a[0:3]\n"
    "v_mfma_f32_16x16x32_bf16 a[0:3], v[208:211], a[192:195], a[0:3]\n"
    "s_waitcnt lgkmcnt(1)\n"
    "v_mfma_f32_16x16x32_bf16 a[0:3], v[204:207], a[196:199], a[0:3]\n"
    "buffer_load_dwordx4 v3, s[44:47], s63 offen offset:2048 lds\n"
    "v_mfma_f32_16x16x32_bf16 a[16:19], v[204:207], a[200:203], a[16:19]\n"
    "v_mfma_f32_16x16x32_bf16 a[16:19], v[208:211], a[196:199], a[16:19]\n"
    "v_mfma_f32_16x16x32_bf16 a[16:19], v[120:123], a[192:195], a[16:19]\n"
    "s_waitcnt vmcnt(19)\n"
    "ds_read_b64_tr_b16 v[168:169], v5 offset:4096\n"
    "v_mfma_f32_16x16x32_bf16 a[32:35], v[120:123], a[196:199], a[32:35]\n"
    "ds_read_b64_tr_b16 v[170:171], v5 offset:4608\n"
    "buffer_load_dwordx4 v4, s[44:47], s63 offen offset:3072 lds\n"
    "v_mfma_f32_16x16x32_bf16 a[32:35], v[208:211], a[200:203], a[32:35]\n"
    "ds_read_b64_tr_b16 v[48:49], v6 offset:4096\n"
    "v_mfma_f32_16x16x32_bf16 a[32:35], v[204:207], a[212:215], a[32:35]\n"
    "ds_read_b64_tr_b16 v[50:51], v6 offset:4608\n"
    "v_mfma_f32_16x16x32_bf16 a[48:51], v[204:207], a[208:211], a[48:51]\n"
    "ds_read_b64_tr_b16 v[60:61], v7 offset:4096\n"
    "s_mov_b32 m0, s68\n"
    "v_mfma_f32_16x16x32_bf16 a[48:51], v[208:211], a[212:215], a[48:51]\n"
    "ds_read_b64_tr_b16 v[62:63], v7 offset:4608\n"
    "s_waitcnt lgkmcnt(6)\n"
    "buffer_load_dwordx4 v1, s[44:47], s64 offen offset:0 lds\n"
    "v_mfma_f32_16x16x32_bf16 a[48:51], v[120:123], a[200:203], a[48:51]\n"
    "ds_read_b64_tr_b16 v[212:213], v8 offset:4096\n"
    "v_mfma_f32_16x16x32_bf16 a[64:67], v[120:123], a[212:215], a[64:67]\n"
    "ds_read_b64_tr_b16 v[214:215], v8 offset:4608\n"
    "v_mfma_f32_16x16x32_bf16 a[64:67], v[208:211], a[208:211], a[64:67]\n"
    "v_mfma_f32_16x16x32_bf16 a[64:67], v[204:207], a[204:207], a[64:67]\n"
    "buffer_load_dwordx4 v2, s[44:47], s64 offen offset:1024 lds\n"
    "v_mfma_f32_16x16x32_bf16 a[80:83], v[204:207], a[216:219], a[80:83]\n"
    "v_mfma_f32_16x16x32_bf16 a[80:83], v[208:211], a[204:207], a[80:83]\n"
    "v_mfma_f32_16x16x32_bf16 a[80:83], v[120:123], a[208:211], a[80:83]\n"
    "v_mfma_f32_16x16x32_bf16 a[96:99], v[120:123], a[204:207], a[96:99]\n"
    "buffer_load_dwordx4 v3, s[44:47], s64 offen offset:2048 lds\n"
    "v_mfma_f32_16x16x32_bf16 a[96:99], v[208:211], a[216:219], a[96:99]\n"
    "v_mfma_f32_16x16x32_bf16 a[96:99], v[204:207], a[220:223], a[96:99]\n"
    "v_mfma_f32_16x16x32_bf16 a[112:115], v[204:207], a[224:227], a[112:115]\n"
    "s_waitcnt vmcnt(19)\n"
    "ds_read_b64_tr_b16 v[244:245], v5 offset:8192\n"
    "v_mfma_f32_16x16x32_bf16 a[112:115], v[208:211], a[220:223], a[112:115]\n"
    "ds_read_b64_tr_b16 v[246:247], v5 offset:8704\n"
    "buffer_load_dwordx4 v4, s[44:47], s64 offen offset:3072 lds\n"
    "v_mfma_f32_16x16x32_bf16 a[112:115], v[120:123], a[216:219], a[112:115]\n"
    "ds_read_b64_tr_b16 v[44:45], v6 offset:8192\n"
    "v_mfma_f32_16x16x32_bf16 a[4:7], v[224:227], a[188:191], a[4:7]\n"
    "ds_read_b64_tr_b16 v[46:47], v6 offset:8704\n"
    "v_mfma_f32_16x16x32_bf16 a[4:7], v[220:223], a[192:195], a[4:7]\n"
    "ds_read_b64_tr_b16 v[196:197], v7 offset:8192\n"
    "s_mov_b32 m0, s69\n"
    "v_mfma_f32_16x16x32_bf16 a[4:7], v[184:187], a[196:199], a[4:7]\n"
    "ds_read_b64_tr_b16 v[198:199], v7 offset:8704\n"
    "s_waitcnt lgkmcnt(6)\n"
    "buffer_load_dwordx4 v1, s[44:47], s65 offen offset:0 lds\n"
    "v_mfma_f32_16x16x32_bf16 a[20:23], v[184:187], a[200:203], a[20:23]\n"
    "ds_read_b64_tr_b16 v[68:69], v8 offset:8192\n"
    "v_mfma_f32_16x16x32_bf16 a[20:23], v[220:223], a[196:199], a[20:23]\n"
    "ds_read_b64_tr_b16 v[70:71], v8 offset:8704\n"
    "v_mfma_f32_16x16x32_bf16 a[20:23], v[224:227], a[192:195], a[20:23]\n"
    "v_mfma_f32_16x16x32_bf16 a[36:39], v[224:227], a[196:199], a[36:39]\n"
    "buffer_load_dwordx4 v2, s[44:47], s65 offen offset:1024 lds\n"
    "v_mfma_f32_16x16x32_bf16 a[36:39], v[220:223], a[200:203], a[36:39]\n"
    "v_mfma_f32_16x16x32_bf16 a[36:39], v[184:187], a[212:215], a[36:39]\n"
    "v_mfma_f32_16x16x32_bf16 a[52:55], v[184:187], a[208:211], a[52:55]\n"
    "v_mfma_f32_16x16x32_bf16 a[52:55], v[220:223], a[212:215], a[52:55]\n"
    "buffer_load_dwordx4 v3, s[44:47], s65 offen offset:2048 lds\n"
    "v_mfma_f32_16x16x32_bf16 a[52:55], v[224:227], a[200:203], a[52:55]\n"
    "v_mfma_f32_16x16x32_bf16 a[68:71], v[224:227], a[212:215], a[68:71]\n"
    "v_mfma_f32_16x16x32_bf16 a[68:71], v[220:223], a[208:211], a[68:71]\n"
    "v_mfma_f32_16x16x32_bf16 a[68:71], v[184:187], a[204:207], a[68:71]\n"
    "buffer_load_dwordx4 v4, s[44:47], s65 offen offset:3072 lds\n"
    "v_mfma_f32_16x16x32_bf16 a[84:87], v[184:187], a[216:219], a[84:87]\n"
    "v_mfma_f32_16x16x32_bf16 a[84:87], v[220:223], a[204:207], a[84:87]\n"
    "v_mfma_f32_16x16x32_bf16 a[84:87], v[224:227], a[208:211], a[84:87]\n"
    "s_mov_b32 m0, s70\n"
    "v_mfma_f32_16x16x32_bf16 a[100:103], v[224:227], a[204:207], a[100:103]\n"
    "s_waitcnt lgkmcnt(0)\n"
    "buffer_load_dwordx4 v1, s[44:47], s66 offen offset:0 lds\n"
    "v_mfma_f32_16x16x32_bf16 a[100:103], v[220:223], a[216:219], a[100:103]\n"
    "v_mfma_f32_16x16x32_bf16 a[100:103], v[184:187], a[220:223], a[100:103]\n"
    "v_mfma_f32_16x16x32_bf16 a[116:119], v[184:187], a[224:227], a[116:119]\n"
    "v_mfma_f32_16x16x32_bf16 a[116:119], v[220:223], a[220:223], a[116:119]\n"
    "buffer_load_dwordx4 v2, s[44:47], s66 offen offset:1024 lds\n"
    "v_mfma_f32_16x16x32_bf16 a[116:119], v[224:227], a[216:219], a[116:119]\n"
    "v_mfma_f32_16x16x32_bf16 a[8:11], v[152:155], a[188:191], a[8:11]\n"
    "v_mfma_f32_16x16x32_bf16 a[8:11], v[148:151], a[192:195], a[8:11]\n"
    "v_mfma_f32_16x16x32_bf16 a[8:11], v[228:231], a[196:199], a[8:11]\n"
    "buffer_load_dwordx4 v3, s[44:47], s66 offen offset:2048 lds\n"
    "v_mfma_f32_16x16x32_bf16 a[24:27], v[228:231], a[200:203], a[24:27]\n"
    "v_mfma_f32_16x16x32_bf16 a[24:27], v[148:151], a[196:199], a[24:27]\n"
    "v_mfma_f32_16x16x32_bf16 a[24:27], v[152:155], a[192:195], a[24:27]\n"
    "v_mfma_f32_16x16x32_bf16 a[40:43], v[152:155], a[196:199], a[40:43]\n"
    "buffer_load_dwordx4 v4, s[44:47], s66 offen offset:3072 lds\n"
    "v_mfma_f32_16x16x32_bf16 a[40:43], v[148:151], a[200:203], a[40:43]\n"
    "v_mfma_f32_16x16x32_bf16 a[40:43], v[228:231], a[212:215], a[40:43]\n"
    "v_mfma_f32_16x16x32_bf16 a[56:59], v[228:231], a[208:211], a[56:59]\n"
    "v_mfma_f32_16x16x32_bf16 a[56:59], v[148:151], a[212:215], a[56:59]\n"
    "v_mfma_f32_16x16x32_bf16 a[56:59], v[152:155], a[200:203], a[56:59]\n"
    "v_mfma_f32_16x16x32_bf16 a[72:75], v[152:155], a[212:215], a[72:75]\n"
    "v_lshlrev_b32 v26, v233, 1\n"
    "v_lshl_or_b32 v26, 1, v235, v26\n"
    "v_mfma_f32_16x16x32_bf16 a[72:75], v[148:151], a[208:211], a[72:75]\n"
    "v_add_u32 v22, 262144, v22\n"
    "v_lshl_or_b32 v26, 1, v201, v26\n"
    "v_mfma_f32_16x16x32_bf16 a[72:75], v[228:231], a[204:207], a[72:75]\n"
    "buffer_load_dword v23, v22, s[44:47], s52 offen\n"
    "v_lshl_or_b32 v26, 1, v203, v26\n"
    "v_mfma_f32_16x16x32_bf16 a[88:91], v[228:231], a[216:219], a[88:91]\n"
    "v_lshl_or_b32 v26, 1, v253, v26\n"
    "v_lshl_or_b32 v26, 1, v255, v26\n"
    "v_mfma_f32_16x16x32_bf16 a[88:91], v[148:151], a[204:207], a[88:91]\n"
    "v_lshl_or_b32 v26, 1, v237, v26\n"
    "v_lshl_or_b32 v26, 1, v239, v26\n"
    "v_mfma_f32_16x16x32_bf16 a[88:91], v[152:155], a[208:211], a[88:91]\n"
    "v_not_b32 v26, v26\n"
    "v_ffbl_b32 v26, v26\n"
    "v_mfma_f32_16x16x32_bf16 a[104:107], v[152:155], a[204:207], a[104:107]\n"
    "global_store_dword v25, v26, s[80:81] sc1\n"
    "v_mfma_f32_16x16x32_bf16 a[104:107], v[148:151], a[216:219], a[104:107]\n"
    "v_mfma_f32_16x16x32_bf16 a[104:107], v[228:231], a[220:223], a[104:107]\n"
    "v_mfma_f32_16x16x32_bf16 a[120:123], v[228:231], a[224:227], a[120:123]\n"
    "v_mfma_f32_16x16x32_bf16 a[120:123], v[148:151], a[220:223], a[120:123]\n"
    "buffer_load_dword v23, v22, s[44:47], s59 offen\n"
    "v_mfma_f32_16x16x32_bf16 a[120:123], v[152:155], a[216:219], a[120:123]\n"
    "v_mfma_f32_16x16x32_bf16 a[12:15], v[164:167], a[188:191], a[12:15]\n"
    "v_mfma_f32_16x16x32_bf16 a[12:15], v[192:195], a[192:195], a[12:15]\n"
    "v_mfma_f32_16x16x32_bf16 a[12:15], v[188:191], a[196:199], a[12:15]\n"
    "v_mfma_f32_16x16x32_bf16 a[28:31], v[188:191], a[200:203], a[28:31]\n"
    "v_mfma_f32_16x16x32_bf16 a[28:31], v[192:195], a[196:199], a[28:31]\n"
    "v_mfma_f32_16x16x32_bf16 a[28:31], v[164:167], a[192:195], a[28:31]\n"
    "v_mfma_f32_16x16x32_bf16 a[44:47], v[164:167], a[196:199], a[44:47]\n"
    "v_mfma_f32_16x16x32_bf16 a[44:47], v[192:195], a[200:203], a[44:47]\n"
    "v_mfma_f32_16x16x32_bf16 a[44:47], v[188:191], a[212:215], a[44:47]\n"
    "v_mfma_f32_16x16x32_bf16 a[60:63], v[188:191], a[208:211], a[60:63]\n"
    "v_mfma_f32_16x16x32_bf16 a[60:63], v[192:195], a[212:215], a[60:63]\n"
    "buffer_load_dword v23, v40, s[40:43], 0 offen offset:768\n"
    "v_mfma_f32_16x16x32_bf16 a[60:63], v[164:167], a[200:203], a[60:63]\n"
    "v_mfma_f32_16x16x32_bf16 a[76:79], v[164:167], a[212:215], a[76:79]\n"
    "v_mfma_f32_16x16x32_bf16 a[76:79], v[192:195], a[208:211], a[76:79]\n"
    "v_mfma_f32_16x16x32_bf16 a[76:79], v[188:191], a[204:207], a[76:79]\n"
    "v_mfma_f32_16x16x32_bf16 a[92:95], v[188:191], a[216:219], a[92:95]\n"
    "v_mfma_f32_16x16x32_bf16 a[92:95], v[192:195], a[204:207], a[92:95]\n"
    "v_mfma_f32_16x16x32_bf16 a[92:95], v[164:167], a[208:211], a[92:95]\n"
    "s_waitcnt vmcnt(28)\n"
    "ds_read_b64_tr_b16 v[240:241], v5 offset:12288\n"
    "v_mfma_f32_16x16x32_bf16 a[108:111], v[164:167], a[204:207], a[108:111]\n"
    "ds_read_b64_tr_b16 v[242:243], v5 offset:12800\n"
    "v_mfma_f32_16x16x32_bf16 a[108:111], v[192:195], a[216:219], a[108:111]\n"
    "ds_read_b64_tr_b16 v[216:217], v6 offset:12288\n"
    "v_mfma_f32_16x16x32_bf16 a[108:111], v[188:191], a[220:223], a[108:111]\n"
    "ds_read_b64_tr_b16 v[218:219], v6 offset:12800\n"
    "v_mfma_f32_16x16x32_bf16 a[124:127], v[188:191], a[224:227], a[124:127]\n"
    "ds_read_b64_tr_b16 v[52:53], v7 offset:12288\n"
    "v_mfma_f32_16x16x32_bf16 a[124:127], v[192:195], a[220:223], a[124:127]\n"
    "ds_read_b64_tr_b16 v[54:55], v7 offset:12800\n"
    "v_mfma_f32_16x16x32_bf16 a[124:127], v[164:167], a[216:219], a[124:127]\n"
    "ds_read_b64_tr_b16 v[64:65], v8 offset:12288\n"
    "v_mfma_f32_16x16x32_bf16 a[0:3], v[168:171], a[200:203], a[0:3]\n"
    "ds_read_b64_tr_b16 v[66:67], v8 offset:12800\n"
    "v_mfma_f32_16x16x32_bf16 a[0:3], v[244:247], a[212:215], a[0:3]\n"
    "s_waitcnt lgkmcnt(5)\n"
    "v_mfma_f32_16x16x32_bf16 a[0:3], v[240:243], a[208:211], a[0:3]\n"
    "v_mfma_f32_16x16x32_bf16 a[16:19], v[240:243], a[204:207], a[16:19]\n"
    "v_mfma_f32_16x16x32_bf16 a[16:19], v[244:247], a[208:211], a[16:19]\n"
    "v_mfma_f32_16x16x32_bf16 a[16:19], v[168:171], a[212:215], a[16:19]\n"
    "v_mfma_f32_16x16x32_bf16 a[32:35], v[168:171], a[208:211], a[32:35]\n"
    "L_st4_%=:\n"
    "s_cbranch_execz L_back_%=\n"
    "v_mfma_f32_16x16x32_bf16 a[32:35], v[244:247], a[204:207], a[32:35]\n"
    "v_mfma_f32_16x16x32_bf16 a[32:35], v[240:243], a[216:219], a[32:35]\n"
    "v_mfma_f32_16x16x32_bf16 a[48:51], v[240:243], a[220:223], a[48:51]\n"
    "v_mfma_f32_16x16x32_bf16 a[48:51], v[244:247], a[216:219], a[48:51]\n"
    "v_mfma_f32_16x16x32_bf16 a[48:51], v[168:171], a[204:207], a[48:51]\n"
    "v_mfma_f32_16x16x32_bf16 a[64:67], v[168:171], a[216:219], a[64:67]\n"
    "v_mfma_f32_16x16x32_bf16 a[64:67], v[244:247], a[220:223], a[64:67]\n"
    "v_mfma_f32_16x16x32_bf16 a[64:67], v[240:243], a[224:227], a[64:67]\n"
    "v_mfma_f32_16x16x32_bf16 a[80:83], v[240:243], a[228:231], a[80:83]\n"
    "v_mfma_f32_16x16x32_bf16 a[80:83], v[244:247], a[224:227], a[80:83]\n"
    "v_mfma_f32_16x16x32_bf16 a[80:83], v[168:171], a[220:223], a[80:83]\n"
    "v_mfma_f32_16x16x32_bf16 a[96:99], v[168:171], a[224:227], a[96:99]\n"
    "v_mfma_f32_16x16x32_bf16 a[96:99], v[244:247], a[228:231], a[96:99]\n"
    "v_mfma_f32_16x16x32_bf16 a[96:99], v[240:243], a[232:235], a[96:99]\n"
    "v_mfma_f32_16x16x32_bf16 a[112:115], v[240:243], a[236:239], a[112:115]\n"
    "v_mfma_f32_16x16x32_bf16 a[112:115], v[244:247], a[232:235], a[112:115]\n"
    "v_mfma_f32_16x16x32_bf16 a[112:115], v[168:171], a[228:231], a[112:115]\n"
    "v_mfma_f32_16x16x32_bf16 a[4:7], v[48:51], a[200:203], a[4:7]\n"
    "v_mfma_f32_16x16x32_bf16 a[4:7], v[44:47], a[212:215], a[4:7]\n"
    "s_waitcnt lgkmcnt(0)\n"
    "v_mfma_f32_16x16x32_bf16 a[4:7], v[216:219], a[208:211], a[4:7]\n"
    "v_mfma_f32_16x16x32_bf16 a[20:23], v[216:219], a[204:207], a[20:23]\n"
    "v_mfma_f32_16x16x32_bf16 a[20:23], v[44:47], a[208:211], a[20:23]\n"
    "v_mfma_f32_16x16x32_bf16 a[20:23], v[48:51], a[212:215], a[20:23]\n"
    "v_mfma_f32_16x16x32_bf16 a[36:39], v[48:51], a[208:211], a[36:39]\n"
    "v_mfma_f32_16x16x32_bf16 a[36:39], v[44:47], a[204:207], a[36:39]\n"
    "v_mfma_f32_16x16x32_bf16 a[36:39], v[216:219], a[216:219], a[36:39]\n"
    "v_mfma_f32_16x16x32_bf16 a[52:55], v[216:219], a[220:223], a[52:55]\n"
    "v_mfma_f32_16x16x32_bf16 a[52:55], v[44:47], a[216:219], a[52:55]\n"
    "v_mfma_f32_16x16x32_bf16 a[52:55], v[48:51], a[204:207], a[52:55]\n"
    "v_mfma_f32_16x16x32_bf16 a[68:71], v[48:51], a[216:219], a[68:71]\n"
    "v_mfma_f32_16x16x32_bf16 a[68:71], v[44:47], a[220:223], a[68:71]\n"
    "v_mfma_f32_16x16x32_bf16 a[68:71], v[216:219], a[224:227], a[68:71]\n"
    "v_mfma_f32_16x16x32_bf16 a[84:87], v[216:219], a[228:231], a[84:87]\n"
    "v_mfma_f32_16x16x32_bf16 a[84:87], v[44:47], a[224:227], a[84:87]\n"
    "v_mfma_f32_16x16x32_bf16 a[84:87], v[48:51], a[220:223], a[84:87]\n"
    "v_mfma_f32_16x16x32_bf16 a[100:103], v[48:51], a[224:227], a[100:103]\n"
    "v_mfma_f32_16x16x32_bf16 a[100:103], v[44:47], a[228:231], a[100:103]\n"
    "v_mfma_f32_16x16x32_bf16 a[100:103], v[216:219], a[232:235], a[100:103]\n"
    "v_mfma_f32_16x16x32_bf16 a[116:119], v[216:219], a[236:239], a[116:119]\n"
    "v_mfma_f32_16x16x32_bf16 a[116:119], v[44:47], a[232:235], a[116:119]\n"
    "v_mfma_f32_16x16x32_bf16 a[116:119], v[48:51], a[228:231], a[116:119]\n"
    "v_mfma_f32_16x16x32_bf16 a[8:11], v[60:63], a[200:203], a[8:11]\n"
    "v_mfma_f32_16x16x32_bf16 a[8:11], v[196:199], a[212:215], a[8:11]\n"
    "v_mfma_f32_16x16x32_bf16 a[8:11], v[52:55], a[208:211], a[8:11]\n"
    "v_mfma_f32_16x16x32_bf16 a[24:27], v[52:55], a[204:207], a[24:27]\n"
    "v_mfma_f32_16x16x32_bf16 a[24:27], v[196:199], a[208:211], a[24:27]\n"
    "v_mfma_f32_16x16x32_bf16 a[24:27], v[60:63], a[212:215], a[24:27]\n"
    "v_mfma_f32_16x16x32_bf16 a[40:43], v[60:63], a[208:211], a[40:43]\n"
    "v_mfma_f32_16x16x32_bf16 a[40:43], v[196:199], a[204:207], a[40:43]\n"
    "v_mfma_f32_16x16x32_bf16 a[40:43], v[52:55], a[216:219], a[40:43]\n"
    "v_mfma_f32_16x16x32_bf16 a[56:59], v[52:55], a[220:223], a[56:59]\n"
    "v_mfma_f32_16x16x32_bf16 a[56:59], v[196:199], a[216:219], a[56:59]\n"
    "v_mfma_f32_16x16x32_bf16 a[56:59], v[60:63], a[204:207], a[56:59]\n"
    "v_mfma_f32_16x16x32_bf16 a[72:75], v[60:63], a[216:219], a[72:75]\n"
    "v_mfma_f32_16x16x32_bf16 a[72:75], v[196:199], a[220:223], a[72:75]\n"
    "v_mfma_f32_16x16x32_bf16 a[72:75], v[52:55], a[224:227], a[72:75]\n"
    "v_mfma_f32_16x16x32_bf16 a[88:91], v[52:55], a[228:231], a[88:91]\n"
    "v_mfma_f32_16x16x32_bf16 a[88:91], v[196:199], a[224:227], a[88:91]\n"
    "v_mfma_f32_16x16x32_bf16 a[88:91], v[60:63], a[220:223], a[88:91]\n"
    "v_mfma_f32_16x16x32_bf16 a[104:107], v[60:63], a[224:227], a[104:107]\n"
    "v_mfma_f32_16x16x32_bf16 a[104:107], v[196:199], a[228:231], a[104:107]\n"
    "v_mfma_f32_16x16x32_bf16 a[104:107], v[52:55], a[232:235], a[104:107]\n"
    "v_mfma_f32_16x16x32_bf16 a[120:123], v[52:55], a[236:239], a[120:123]\n"
    "v_mfma_f32_16x16x32_bf16 a[120:123], v[196:199], a[232:235], a[120:123]\n"
    "v_mfma_f32_16x16x32_bf16 a[120:123], v[60:63], a[228:231], a[120:123]\n"
    "s_waitcnt vmcnt(24)\n"
    "ds_read_b64_tr_b16 v[56:57], v5 offset:16384\n"
    "v_mfma_f32_16x16x32_bf16 a[12:15], v[212:215], a[200:203], a[12:15]\n"
    "ds_read_b64_tr_b16 v[58:59], v5 offset:16896\n"
    "v_mfma_f32_16x16x32_bf16 a[12:15], v[68:71], a[212:215], a[12:15]\n"
    "ds_read_b64_tr_b16 v[108:109], v6 offset:16384\n"
    "v_mfma_f32_16x16x32_bf16 a[12:15], v[64:67], a[208:211], a[12:15]\n"
    "ds_read_b64_tr_b16 v[110:111], v6 offset:16896\n"
    "v_mfma_f32_16x16x32_bf16 a[28:31], v[64:67], a[204:207], a[28:31]\n"
    "ds_read_b64_tr_b16 v[112:113], v7 offset:16384\n"
    "v_mfma_f32_16x16x32_bf16 a[28:31], v[68:71], a[208:211], a[28:31]\n"
    "ds_read_b64_tr_b16 v[114:115], v7 offset:16896\n"
    "v_mfma_f32_16x16x32_bf16 a[28:31], v[212:215], a[212:215], a[28:31]\n"
    "ds_read_b64_tr_b16 v[248:249], v8 offset:16384\n"
    "v_mfma_f32_16x16x32_bf16 a[44:47], v[212:215], a[208:211], a[44:47]\n"
    "ds_read_b64_tr_b16 v[250:251], v8 offset:16896\n"
    "v_mfma_f32_16x16x32_bf16 a[44:47], v[68:71], a[204:207], a[44:47]\n"
    "v_mfma_f32_16x16x32_bf16 a[44:47], v[64:67], a[216:219], a[44:47]\n"
    "s_waitcnt vmcnt(20)\n"
    "ds_read_b64_tr_b16 v[80:81], v5 offset:20480\n"
    "v_mfma_f32_16x16x32_bf16 a[60:63], v[64:67], a[220:223], a[60:63]\n"
    "ds_read_b64_tr_b16 v[82:83], v5 offset:20992\n"
    "v_mfma_f32_16x16x32_bf16 a[60:63], v[68:71], a[216:219], a[60:63]\n"
    "ds_read_b64_tr_b16 v[124:125], v6 offset:20480\n"
    "v_mfma_f32_16x16x32_bf16 a[60:63], v[212:215], a[204:207], a[60:63]\n"
    "ds_read_b64_tr_b16 v[126:127], v6 offset:20992\n"
    "v_mfma_f32_16x16x32_bf16 a[76:79], v[212:215], a[216:219], a[76:79]\n"
    "ds_read_b64_tr_b16 v[128:129], v7 offset:20480\n"
    "v_mfma_f32_16x16x32_bf16 a[76:79], v[68:71], a[220:223], a[76:79]\n"
    "ds_read_b64_tr_b16 v[130:131], v7 offset:20992\n"
    "v_mfma_f32_16x16x32_bf16 a[76:79], v[64:67], a[224:227], a[76:79]\n"
    "ds_read_b64_tr_b16 v[132:133], v8 offset:20480\n"
    "v_mfma_f32_16x16x32_bf16 a[92:95], v[64:67], a[228:231], a[92:95]\n"
    "ds_read_b64_tr_b16 v[134:135], v8 offset:20992\n"
    "v_mfma_f32_16x16x32_bf16 a[92:95], v[68:71], a[224:227], a[92:95]\n"
    "v_mfma_f32_16x16x32_bf16 a[92:95], v[212:215], a[220:223], a[92:95]\n"
    "s_waitcnt vmcnt(16)\n"
    "ds_read_b64_tr_b16 v[76:77], v5 offset:24576\n"
    "v_mfma_f32_16x16x32_bf16 a[108:111], v[212:215], a[224:227], a[108:111]\n"
    "ds_read_b64_tr_b16 v[78:79], v5 offset:25088\n"
    "v_mfma_f32_16x16x32_bf16 a[108:111], v[68:71], a[228:231], a[108:111]\n"
    "ds_read_b64_tr_b16 v[88:89], v6 offset:24576\n"
    "v_mfma_f32_16x16x32_bf16 a[108:111], v[64:67], a[232:235], a[108:111]\n"
    "ds_read_b64_tr_b16 v[90:91], v6 offset:25088\n"
    "v_mfma_f32_16x16x32_bf16 a[124:127], v[64:67], a[236:239], a[124:127]\n"
    "ds_read_b64_tr_b16 v[92:93], v7 offset:24576\n"
    "v_mfma_f32_16x16x32_bf16 a[124:127], v[68:71], a[232:235], a[124:127]\n"
    "ds_read_b64_tr_b16 v[94:95], v7 offset:25088\n"
    "v_mfma_f32_16x16x32_bf16 a[124:127], v[212:215], a[228:231], a[124:127]\n"
    "ds_read_b64_tr_b16 v[96:97], v8 offset:24576\n"
    "s_waitcnt lgkmcnt(6)\n"
    "v_mfma_f32_16x16x32_bf16 a[0:3], v[56:59], a[204:207], a[0:3]\n"
    "ds_read_b64_tr_b16 v[98:99], v8 offset:25088\n"
    "v_mfma_f32_16x16x32_bf16 a[0:3], v[80:83], a[216:219], a[0:3]\n"
    "s_waitcnt lgkmcnt(5)\n"
    "v_mfma_f32_16x16x32_bf16 a[0:3], v[76:79], a[220:223], a[0:3]\n"
    "v_mfma_f32_16x16x32_bf16 a[16:19], v[76:79], a[224:227], a[16:19]\n"
    "v_mfma_f32_16x16x32_bf16 a[16:19], v[80:83], a[220:223], a[16:19]\n"
    "v_mfma_f32_16x16x32_bf16 a[16:19], v[56:59], a[216:219], a[16:19]\n"
    "v_mfma_f32_16x16x32_bf16 a[32:35], v[56:59], a[220:223], a[32:35]\n"
    "v_mfma_f32_16x16x32_bf16 a[32:35], v[80:83], a[224:227], a[32:35]\n"
    "v_mfma_f32_16x16x32_bf16 a[32:35], v[76:79], a[228:231], a[32:35]\n"
    "v_mfma_f32_16x16x32_bf16 a[48:51], v[76:79], a[232:235], a[48:51]\n"
    "v_mfma_f32_16x16x32_bf16 a[48:51], v[80:83], a[228:231], a[48:51]\n"
    "v_mfma_f32_16x16x32_bf16 a[48:51], v[56:59], a[224:227], a[48:51]\n"
    "v_mfma_f32_16x16x32_bf16 a[64:67], v[56:59], a[228:231], a[64:67]\n"
    "v_mfma_f32_16x16x32_bf16 a[64:67], v[80:83], a[232:235], a[64:67]\n"
    "v_mfma_f32_16x16x32_bf16 a[64:67], v[76:79], a[236:239], a[64:67]\n"
    "v_mfma_f32_16x16x32_bf16 a[80:83], v[76:79], a[240:243], a[80:83]\n"
    "v_mfma_f32_16x16x32_bf16 a[80:83], v[80:83], a[236:239], a[80:83]\n"
    "v_mfma_f32_16x16x32_bf16 a[80:83], v[56:59], a[232:235], a[80:83]\n"
    "v_mfma_f32_16x16x32_bf16 a[96:99], v[56:59], a[236:239], a[96:99]\n"
    "v_mfma_f32_16x16x32_bf16 a[96:99], v[80:83], a[240:243], a[96:99]\n"
    "v_mfma_f32_16x16x32_bf16 a[96:99], v[76:79], a[244:247], a[96:99]\n"
    "v_mfma_f32_16x16x32_bf16 a[112:115], v[76:79], a[248:251], a[112:115]\n"
    "s_mov_b32 m0, s71\n"
    "v_mfma_f32_16x16x32_bf16 a[112:115], v[80:83], a[244:247], a[112:115]\n"
    "v_add_u32 v3, 0x40000, v3\n"
    "v_add_u32 v4, 0x40000, v4\n"
    "v_mfma_f32_16x16x32_bf16 a[112:115], v[56:59], a[240:243], a[112:115]\n"
    "v_add_u32 v1, 0x40000, v1\n"
    "v_add_u32 v2, 0x40000, v2\n"
    "v_mfma_f32_16x16x32_bf16 a[4:7], v[108:111], a[204:207], a[4:7]\n"
    "buffer_load_dwordx4 v1, s[44:47], s52 offen offset:0 lds\n"
    "v_mfma_f32_16x16x32_bf16 a[4:7], v[124:127], a[216:219], a[4:7]\n"
    "s_waitcnt lgkmcnt(0)\n"
    "v_mfma_f32_16x16x32_bf16 a[4:7], v[88:91], a[220:223], a[4:7]\n"
    "v_mfma_f32_16x16x32_bf16 a[20:23], v[88:91], a[224:227], a[20:23]\n"
    "v_mfma_f32_16x16x32_bf16 a[20:23], v[124:127], a[220:223], a[20:23]\n"
    "buffer_load_dwordx4 v2, s[44:47], s52 offen offset:1024 lds\n"
    "v_mfma_f32_16x16x32_bf16 a[20:23], v[108:111], a[216:219], a[20:23]\n"
    "v_mfma_f32_16x16x32_bf16 a[36:39], v[108:111], a[220:223], a[36:39]\n"
    "v_mfma_f32_16x16x32_bf16 a[36:39], v[124:127], a[224:227], a[36:39]\n"
    "v_mfma_f32_16x16x32_bf16 a[36:39], v[88:91], a[228:231], a[36:39]\n"
    "buffer_load_dwordx4 v3, s[44:47], s52 offen offset:2048 lds\n"
    "v_mfma_f32_16x16x32_bf16 a[52:55], v[88:91], a[232:235], a[52:55]\n"
    "v_mfma_f32_16x16x32_bf16 a[52:55], v[124:127], a[228:231], a[52:55]\n"
    "v_mfma_f32_16x16x32_bf16 a[52:55], v[108:111], a[224:227], a[52:55]\n"
    "v_mfma_f32_16x16x32_bf16 a[68:71], v[108:111], a[228:231], a[68:71]\n"
    "buffer_load_dwordx4 v4, s[44:47], s52 offen offset:3072 lds\n"
    "v_mfma_f32_16x16x32_bf16 a[68:71], v[124:127], a[232:235], a[68:71]\n"
    "v_mfma_f32_16x16x32_bf16 a[68:71], v[88:91], a[236:239], a[68:71]\n"
    "v_mfma_f32_16x16x32_bf16 a[84:87], v[88:91], a[240:243], a[84:87]\n"
    "s_add_u32 m0, s75, 27904\n"
    "v_mfma_f32_16x16x32_bf16 a[84:87], v[124:127], a[236:239], a[84:87]\n"
    "buffer_load_dwordx4 v12, s[40:43], 0 offen offset:768 lds\n"
    "v_mfma_f32_16x16x32_bf16 a[84:87], v[108:111], a[232:235], a[84:87]\n"
    "v_mfma_f32_16x16x32_bf16 a[100:103], v[108:111], a[236:239], a[100:103]\n"
    "v_mfma_f32_16x16x32_bf16 a[100:103], v[124:127], a[240:243], a[100:103]\n"
    "s_add_u32 m0, s75, 28928\n"
    "v_mfma_f32_16x16x32_bf16 a[100:103], v[88:91], a[244:247], a[100:103]\n"
    "buffer_load_dwordx4 v13, s[40:43], 0 offen offset:768 lds\n"
    "v_mfma_f32_16x16x32_bf16 a[116:119], v[88:91], a[248:251], a[116:119]\n"
    "v_mfma_f32_16x16x32_bf16 a[116:119], v[124:127], a[244:247], a[116:119]\n"
    "v_mfma_f32_16x16x32_bf16 a[116:119], v[108:111], a[240:243], a[116:119]\n"
    "s_add_u32 m0, s75, 29952\n"
    "v_mfma_f32_16x16x32_bf16 a[8:11], v[112:115], a[204:207], a[8:11]\n"
    "buffer_load_dwordx4 v14, s[40:43], 0 offen offset:768 lds\n"
    "v_mfma_f32_16x16x32_bf16 a[8:11], v[128:131], a[216:219], a[8:11]\n"
    "v_mfma_f32_16x16x32_bf16 a[8:11], v[92:95], a[220:223], a[8:11]\n"
    "v_mfma_f32_16x16x32_bf16 a[24:27], v[92:95], a[224:227], a[24:27]\n"
    "s_add_u32 m0, s75, 30976\n"
    "v_mfma_f32_16x16x32_bf16 a[24:27], v[128:131], a[220:223], a[24:27]\n"
    "buffer_load_dwordx4 v15, s[40:43], 0 offen offset:768 lds\n"
    "v_mfma_f32_16x16x32_bf16 a[24:27], v[112:115], a[216:219], a[24:27]\n"
    "v_mfma_f32_16x16x32_bf16 a[40:43], v[112:115], a[220:223], a[40:43]\n"
    "v_mfma_f32_16x16x32_bf16 a[40:43], v[128:131], a[224:227], a[40:43]\n"
    "s_mov_b32 m0, s72\n"
    "v_mfma_f32_16x16x32_bf16 a[40:43], v[92:95], a[228:231], a[40:43]\n"
    "buffer_load_dwordx4 v1, s[44:47], s53 offen offset:0 lds\n"
    "v_mfma_f32_16x16x32_bf16 a[56:59], v[92:95], a[232:235], a[56:59]\n"
    "v_mfma_f32_16x16x32_bf16 a[56:59], v[128:131], a[228:231], a[56:59]\n"
    "v_mfma_f32_16x16x32_bf16 a[56:59], v[112:115], a[224:227], a[56:59]\n"
    "v_mfma_f32_16x16x32_bf16 a[72:75], v[112:115], a[228:231], a[72:75]\n"
    "buffer_load_dwordx4 v2, s[44:47], s53 offen offset:1024 lds\n"
    "v_mfma_f32_16x16x32_bf16 a[72:75], v[128:131], a[232:235], a[72:75]\n"
    "v_mfma_f32_16x16x32_bf16 a[72:75], v[92:95], a[236:239], a[72:75]\n"
    "v_mfma_f32_16x16x32_bf16 a[88:91], v[92:95], a[240:243], a[88:91]\n"
    "v_mfma_f32_16x16x32_bf16 a[88:91], v[128:131], a[236:239], a[88:91]\n"
    "buffer_load_dwordx4 v3, s[44:47], s53 offen offset:2048 lds\n"
    "v_mfma_f32_16x16x32_bf16 a[88:91], v[112:115], a[232:235], a[88:91]\n"
    "v_mfma_f32_16x16x32_bf16 a[104:107], v[112:115], a[236:239], a[104:107]\n"
    "v_mfma_f32_16x16x32_bf16 a[104:107], v[128:131], a[240:243], a[104:107]\n"
    "v_mfma_f32_16x16x32_bf16 a[104:107], v[92:95], a[244:247], a[104:107]\n"
    "buffer_load_dwordx4 v4, s[44:47], s53 offen offset:3072 lds\n"
    "v_mfma_f32_16x16x32_bf16 a[120:123], v[92:95], a[248:251], a[120:123]\n"
    "v_mfma_f32_16x16x32_bf16 a[120:123], v[128:131], a[244:247], a[120:123]\n"
    "v_mfma_f32_16x16x32_bf16 a[120:123], v[112:115], a[240:243], a[120:123]\n"
    "s_waitcnt vmcnt(24)\n"
    "ds_read_b64_tr_b16 v[72:73], v5 offset:0\n"
    "s_add_u32 m0, s75, 32000\n"
    "v_mfma_f32_16x16x32_bf16 a[12:15], v[248:251], a[204:207], a[12:15]\n"
    "ds_read_b64_tr_b16 v[74:75], v5 offset:512\n"
    "buffer_load_dwordx4 v16, s[40:43], 0 offen offset:768 lds\n"
    "v_mfma_f32_16x16x32_bf16 a[12:15], v[132:135], a[216:219], a[12:15]\n"
    "ds_read_b64_tr_b16 v[156:157], v6 offset:0\n"
    "v_mfma_f32_16x16x32_bf16 a[12:15], v[96:99], a[220:223], a[12:15]\n"
    "ds_read_b64_tr_b16 v[158:159], v6 offset:512\n"
    "v_mfma_f32_16x16x32_bf16 a[28:31], v[96:99], a[224:227], a[28:31]\n"
    "ds_read_b64_tr_b16 v[160:161], v7 offset:0\n"
    "s_add_u32 m0, s75, 33024\n"
    "v_mfma_f32_16x16x32_bf16 a[28:31], v[132:135], a[220:223], a[28:31]\n"
    "ds_read_b64_tr_b16 v[162:163], v7 offset:512\n"
    "buffer_load_dwordx4 v17, s[40:43], 0 offen offset:768 lds\n"
    "v_mfma_f32_16x16x32_bf16 a[28:31], v[248:251], a[216:219], a[28:31]\n"
    "ds_read_b64_tr_b16 v[84:85], v8 offset:0\n"
    "v_mfma_f32_16x16x32_bf16 a[44:47], v[248:251], a[220:223], a[44:47]\n"
    "ds_read_b64_tr_b16 v[86:87], v8 offset:512\n"
    "v_mfma_f32_16x16x32_bf16 a[44:47], v[132:135], a[224:227], a[44:47]\n"
    "s_add_u32 m0, s75, 34048\n"
    "v_mfma_f32_16x16x32_bf16 a[44:47], v[96:99], a[228:231], a[44:47]\n"
    "s_waitcnt vmcnt(22)\n"
    "ds_read_b64_tr_b16 v[136:137], v5 offset:4096\n"
    "buffer_load_dwordx4 v18, s[40:43], 0 offen offset:768 lds\n"
    "v_mfma_f32_16x16x32_bf16 a[60:63], v[96:99], a[232:235], a[60:63]\n"
    "ds_read_b64_tr_b16 v[138:139], v5 offset:4608\n"
    "v_mfma_f32_16x16x32_bf16 a[60:63], v[132:135], a[228:231], a[60:63]\n"
    "ds_read_b64_tr_b16 v[172:173], v6 offset:4096\n"
    "v_mfma_f32_16x16x32_bf16 a[60:63], v[248:251], a[224:227], a[60:63]\n"
    "ds_read_b64_tr_b16 v[174:175], v6 offset:4608\n"
    "s_add_u32 m0, s75, 35072\n"
    "v_mfma_f32_16x16x32_bf16 a[76:79], v[248:251], a[228:231], a[76:79]\n"
    "ds_read_b64_tr_b16 v[176:177], v7 offset:4096\n"
    "buffer_load_dwordx4 v19, s[40:43], 0 offen offset:768 lds\n"
    "v_mfma_f32_16x16x32_bf16 a[76:79], v[132:135], a[232:235], a[76:79]\n"
    "ds_read_b64_tr_b16 v[178:179], v7 offset:4608\n"
    "v_mfma_f32_16x16x32_bf16 a[76:79], v[96:99], a[236:239], a[76:79]\n"
    "ds_read_b64_tr_b16 v[180:181], v8 offset:4096\n"
    "v_mfma_f32_16x16x32_bf16 a[92:95], v[96:99], a[240:243], a[92:95]\n"
    "ds_read_b64_tr_b16 v[182:183], v8 offset:4608\n"
    "s_mov_b32 m0, s73\n"
    "v_mfma_f32_16x16x32_bf16 a[92:95], v[132:135], a[236:239], a[92:95]\n"
    "buffer_load_dwordx4 v1, s[44:47], s54 offen offset:0 lds\n"
    "v_mfma_f32_16x16x32_bf16 a[92:95], v[248:251], a[232:235], a[92:95]\n"
    "s_waitcnt vmcnt(21)\n"
    "ds_read_b64_tr_b16 v[100:101], v5 offset:8192\n"
    "v_mfma_f32_16x16x32_bf16 a[108:111], v[248:251], a[236:239], a[108:111]\n"
    "ds_read_b64_tr_b16 v[102:103], v5 offset:8704\n"
    "v_mfma_f32_16x16x32_bf16 a[108:111], v[132:135], a[240:243], a[108:111]\n"
    "ds_read_b64_tr_b16 v[104:105], v6 offset:8192\n"
    "v_mfma_f32_16x16x32_bf16 a[108:111], v[96:99], a[244:247], a[108:111]\n"
    "ds_read_b64_tr_b16 v[106:107], v6 offset:8704\n"
    "buffer_load_dwordx4 v2, s[44:47], s54 offen offset:1024 lds\n"
    "v_mfma_f32_16x16x32_bf16 a[124:127], v[96:99], a[248:251], a[124:127]\n"
    "ds_read_b64_tr_b16 v[140:141], v7 offset:8192\n"
    "v_mfma_f32_16x16x32_bf16 a[124:127], v[132:135], a[244:247], a[124:127]\n"
    "ds_read_b64_tr_b16 v[142:143], v7 offset:8704\n"
    "v_mfma_f32_16x16x32_bf16 a[124:127], v[248:251], a[240:243], a[124:127]\n"
    "ds_read_b64_tr_b16 v[144:145], v8 offset:8192\n"
    "s_waitcnt lgkmcnt(6)\n"
    "v_mfma_f32_16x16x32_bf16 a[0:3], v[72:75], a[224:227], a[0:3]\n"
    "ds_read_b64_tr_b16 v[146:147], v8 offset:8704\n"
    "buffer_load_dwordx4 v3, s[44:47], s54 offen offset:2048 lds\n"
    "v_mfma_f32_16x16x32_bf16 a[0:3], v[136:139], a[228:231], a[0:3]\n"
    "s_waitcnt lgkmcnt(5)\n"
    "v_mfma_f32_16x16x32_bf16 a[0:3], v[100:103], a[232:235], a[0:3]\n"
    "v_mfma_f32_16x16x32_bf16 a[16:19], v[100:103], a[236:239], a[16:19]\n"
    "v_mfma_f32_16x16x32_bf16 a[16:19], v[136:139], a[232:235], a[16:19]\n"
    "buffer_load_dwordx4 v4, s[44:47], s54 offen offset:3072 lds\n"
    "v_mfma_f32_16x16x32_bf16 a[16:19], v[72:75], a[228:231], a[16:19]\n"
    "v_mfma_f32_16x16x32_bf16 a[32:35], v[72:75], a[232:235], a[32:35]\n"
    "v_mfma_f32_16x16x32_bf16 a[32:35], v[136:139], a[236:239], a[32:35]\n"
    "s_add_u32 m0, s75, 36096\n"
    "v_mfma_f32_16x16x32_bf16 a[32:35], v[100:103], a[240:243], a[32:35]\n"
    "buffer_load_dwordx4 v20, s[40:43], 0 offen offset:768 lds\n"
    "v_mfma_f32_16x16x32_bf16 a[48:51], v[100:103], a[244:247], a[48:51]\n"
    "v_mfma_f32_16x16x32_bf16 a[48:51], v[136:139], a[240:243], a[48:51]\n"
    "v_mfma_f32_16x16x32_bf16 a[48:51], v[72:75], a[236:239], a[48:51]\n"
    "s_add_u32 m0, s75, 37120\n"
    "v_mfma_f32_16x16x32_bf16 a[64:67], v[72:75], a[240:243], a[64:67]\n"
    "buffer_load_dwordx4 v21, s[40:43], 0 offen offset:768 lds\n"
    "v_mfma_f32_16x16x32_bf16 a[64:67], v[136:139], a[244:247], a[64:67]\n"
    "v_mfma_f32_16x16x32_bf16 a[64:67], v[100:103], a[248:251], a[64:67]\n"
    "v_mfma_f32_16x16x32_bf16 a[80:83], v[100:103], a[252:255], a[80:83]\n"
    "s_mov_b32 m0, s74\n"
    "v_mfma_f32_16x16x32_bf16 a[80:83], v[136:139], a[248:251], a[80:83]\n"
    "buffer_load_dwordx4 v1, s[44:47], s55 offen offset:0 lds\n"
    "v_mfma_f32_16x16x32_bf16 a[80:83], v[72:75], a[244:247], a[80:83]\n"
    "v_mfma_f32_16x16x32_bf16 a[96:99], v[72:75], a[248:251], a[96:99]\n"
    "v_mfma_f32_16x16x32_bf16 a[96:99], v[136:139], a[252:255], a[96:99]\n"
    "v_mfma_f32_16x16x32_bf16 a[96:99], v[100:103], a[128:131], a[96:99]\n"
    "buffer_load_dwordx4 v2, s[44:47], s55 offen offset:1024 lds\n"
    "v_mfma_f32_16x16x32_bf16 a[112:115], v[100:103], a[132:135], a[112:115]\n"
    "v_mfma_f32_16x16x32_bf16 a[112:115], v[136:139], a[128:131], a[112:115]\n"
    "v_mfma_f32_16x16x32_bf16 a[112:115], v[72:75], a[252:255], a[112:115]\n"
    "v_mfma_f32_16x16x32_bf16 a[4:7], v[156:159], a[224:227], a[4:7]\n"
    "buffer_load_dwordx4 v3, s[44:47], s55 offen offset:2048 lds\n"
    "v_mfma_f32_16x16x32_bf16 a[4:7], v[172:175], a[228:231], a[4:7]\n"
    "s_waitcnt lgkmcnt(0)\n"
    "v_mfma_f32_16x16x32_bf16 a[4:7], v[104:107], a[232:235], a[4:7]\n"
    "v_mfma_f32_16x16x32_bf16 a[20:23], v[104:107], a[236:239], a[20:23]\n"
    "v_mfma_f32_16x16x32_bf16 a[20:23], v[172:175], a[232:235], a[20:23]\n"
    "buffer_load_dwordx4 v4, s[44:47], s55 offen offset:3072 lds\n"
    "v_mfma_f32_16x16x32_bf16 a[20:23], v[156:159], a[228:231], a[20:23]\n"
    "v_mfma_f32_16x16x32_bf16 a[36:39], v[156:159], a[232:235], a[36:39]\n"
    "v_mfma_f32_16x16x32_bf16 a[36:39], v[172:175], a[236:239], a[36:39]\n"
    "s_mov_b32 m0, s68\n"
    "v_mfma_f32_16x16x32_bf16 a[36:39], v[104:107], a[240:243], a[36:39]\n"
    "buffer_load_dwordx4 v1, s[44:47], s56 offen offset:0 lds\n"
    "v_mfma_f32_16x16x32_bf16 a[52:55], v[104:107], a[244:247], a[52:55]\n"
    "v_mfma_f32_16x16x32_bf16 a[52:55], v[172:175], a[240:243], a[52:55]\n"
    "v_mfma_f32_16x16x32_bf16 a[52:55], v[156:159], a[236:239], a[52:55]\n"
    "v_mfma_f32_16x16x32_bf16 a[68:71], v[156:159], a[240:243], a[68:71]\n"
    "buffer_load_dwordx4 v2, s[44:47], s56 offen offset:1024 lds\n"
    "v_mfma_f32_16x16x32_bf16 a[68:71], v[172:175], a[244:247], a[68:71]\n"
    "v_mfma_f32_16x16x32_bf16 a[68:71], v[104:107], a[248:251], a[68:71]\n"
    "v_mfma_f32_16x16x32_bf16 a[84:87], v[104:107], a[252:255], a[84:87]\n"
    "v_mfma_f32_16x16x32_bf16 a[84:87], v[172:175], a[248:251], a[84:87]\n"
    "buffer_load_dwordx4 v3, s[44:47], s56 offen offset:2048 lds\n"
    "v_mfma_f32_16x16x32_bf16 a[84:87], v[156:159], a[244:247], a[84:87]\n"
    "v_mfma_f32_16x16x32_bf16 a[100:103], v[156:159], a[248:251], a[100:103]\n"
    "v_mfma_f32_16x16x32_bf16 a[100:103], v[172:175], a[252:255], a[100:103]\n"
    "v_mfma_f32_16x16x32_bf16 a[100:103], v[104:107], a[128:131], a[100:103]\n"
    "buffer_load_dwordx4 v4, s[44:47], s56 offen offset:3072 lds\n"
    "v_mfma_f32_16x16x32_bf16 a[116:119], v[104:107], a[132:135], a[116:119]\n"
    "v_mfma_f32_16x16x32_bf16 a[116:119], v[172:175], a[128:131], a[116:119]\n"
    "v_mfma_f32_16x16x32_bf16 a[116:119], v[156:159], a[252:255], a[116:119]\n"
    "s_mov_b32 m0, s69\n"
    "v_mfma_f32_16x16x32_bf16 a[8:11], v[160:163], a[224:227], a[8:11]\n"
    "buffer_load_dwordx4 v1, s[44:47], s57 offen offset:0 lds\n"
    "v_mfma_f32_16x16x32_bf16 a[8:11], v[176:179], a[228:231], a[8:11]\n"
    "v_mfma_f32_16x16x32_bf16 a[8:11], v[140:143], a[232:235], a[8:11]\n"
    "v_mfma_f32_16x16x32_bf16 a[24:27], v[140:143], a[236:239], a[24:27]\n"
    "v_mfma_f32_16x16x32_bf16 a[24:27], v[176:179], a[232:235], a[24:27]\n"
    "buffer_load_dwordx4 v2, s[44:47], s57 offen offset:1024 lds\n"
    "v_mfma_f32_16x16x32_bf16 a[24:27], v[160:163], a[228:231], a[24:27]\n"
    "v_mfma_f32_16x16x32_bf16 a[40:43], v[160:163], a[232:235], a[40:43]\n"
    "v_mfma_f32_16x16x32_bf16 a[40:43], v[176:179], a[236:239], a[40:43]\n"
    "v_mfma_f32_16x16x32_bf16 a[40:43], v[140:143], a[240:243], a[40:43]\n"
    "buffer_load_dwordx4 v3, s[44:47], s57 offen offset:2048 lds\n"
    "v_mfma_f32_16x16x32_bf16 a[56:59], v[140:143], a[244:247], a[56:59]\n"
    "v_mfma_f32_16x16x32_bf16 a[56:59], v[176:179], a[240:243], a[56:59]\n"
    "v_mfma_f32_16x16x32_bf16 a[56:59], v[160:163], a[236:239], a[56:59]\n"
    "s_waitcnt vmcnt(29)\n"
    "ds_read_b64_tr_b16 v[116:117], v5 offset:12288\n"
    "v_mfma_f32_16x16x32_bf16 a[72:75], v[160:163], a[240:243], a[72:75]\n"
    "ds_read_b64_tr_b16 v[118:119], v5 offset:12800\n"
    "buffer_load_dwordx4 v4, s[44:47], s57 offen offset:3072 lds\n"
    "v_mfma_f32_16x16x32_bf16 a[72:75], v[176:179], a[244:247], a[72:75]\n"
    "ds_read_b64_tr_b16 v[204:205], v6 offset:12288\n"
    "v_mfma_f32_16x16x32_bf16 a[72:75], v[140:143], a[248:251], a[72:75]\n"
    "ds_read_b64_tr_b16 v[206:207], v6 offset:12800\n"
    "v_mfma_f32_16x16x32_bf16 a[88:91], v[140:143], a[252:255], a[88:91]\n"
    "ds_read_b64_tr_b16 a[140:141], v7 offset:12288\n"
    "s_mov_b32 m0, s70\n"
    "v_mfma_f32_16x16x32_bf16 a[88:91], v[176:179], a[248:251], a[88:91]\n"
    "ds_read_b64_tr_b16 a[142:143], v7 offset:12800\n"
    "buffer_load_dwordx4 v1, s[44:47], s58 offen offset:0 lds\n"
    "v_mfma_f32_16x16x32_bf16 a[88:91], v[160:163], a[244:247], a[88:91]\n"
    "ds_read_b64_tr_b16 a[144:145], v8 offset:12288\n"
    "v_mfma_f32_16x16x32_bf16 a[104:107], v[160:163], a[248:251], a[104:107]\n"
    "ds_read_b64_tr_b16 a[146:147], v8 offset:12800\n"
    "v_mfma_f32_16x16x32_bf16 a[104:107], v[176:179], a[252:255], a[104:107]\n"
    "v_mfma_f32_16x16x32_bf16 a[104:107], v[140:143], a[128:131], a[104:107]\n"
    "buffer_load_dwordx4 v2, s[44:47], s58 offen offset:1024 lds\n"
    "v_mfma_f32_16x16x32_bf16 a[120:123], v[140:143], a[132:135], a[120:123]\n"
    "v_mfma_f32_16x16x32_bf16 a[120:123], v[176:179], a[128:131], a[120:123]\n"
    "v_mfma_f32_16x16x32_bf16 a[120:123], v[160:163], a[252:255], a[120:123]\n"
    "v_mfma_f32_16x16x32_bf16 a[12:15], v[84:87], a[224:227], a[12:15]\n"
    "buffer_load_dwordx4 v3, s[44:47], s58 offen offset:2048 lds\n"
    "v_mfma_f32_16x16x32_bf16 a[12:15], v[180:183], a[228:231], a[12:15]\n"
    "v_mfma_f32_16x16x32_bf16 a[12:15], v[144:147], a[232:235], a[12:15]\n"
    "v_mfma_f32_16x16x32_bf16 a[28:31], v[144:147], a[236:239], a[28:31]\n"
    "s_waitcnt vmcnt(25)\n"
    "ds_read_b64_tr_b16 v[208:209], v5 offset:16384\n"
    "v_mfma_f32_16x16x32_bf16 a[28:31], v[180:183], a[232:235], a[28:31]\n"
    "ds_read_b64_tr_b16 v[210:211], v5 offset:16896\n"
    "buffer_load_dwordx4 v4, s[44:47], s58 offen offset:3072 lds\n"
    "v_mfma_f32_16x16x32_bf16 a[28:31], v[84:87], a[228:231], a[28:31]\n"
    "ds_read_b64_tr_b16 v[120:121], v6 offset:16384\n"
    "v_mfma_f32_16x16x32_bf16 a[44:47], v[84:87], a[232:235], a[44:47]\n"
    "ds_read_b64_tr_b16 v[122:123], v6 offset:16896\n"
    "v_mfma_f32_16x16x32_bf16 a[44:47], v[180:183], a[236:239], a[44:47]\n"
    "ds_read_b64_tr_b16 a[148:149], v7 offset:16384\n"
    "s_mov_b32 m0, s71\n"
    "v_mfma_f32_16x16x32_bf16 a[44:47], v[144:147], a[240:243], a[44:47]\n"
    "ds_read_b64_tr_b16 a[150:151], v7 offset:16896\n"
    "s_waitcnt lgkmcnt(6)\n"
    "buffer_load_dwordx4 v1, s[44:47], s59 offen offset:0 lds\n"
    "v_mfma_f32_16x16x32_bf16 a[60:63], v[144:147], a[244:247], a[60:63]\n"
    "ds_read_b64_tr_b16 a[156:157], v8 offset:16384\n"
    "v_mfma_f32_16x16x32_bf16 a[60:63], v[180:183], a[240:243], a[60:63]\n"
    "ds_read_b64_tr_b16 a[158:159], v8 offset:16896\n"
    "v_mfma_f32_16x16x32_bf16 a[60:63], v[84:87], a[236:239], a[60:63]\n"
    "v_mfma_f32_16x16x32_bf16 a[76:79], v[84:87], a[240:243], a[76:79]\n"
    "buffer_load_dwordx4 v2, s[44:47], s59 offen offset:1024 lds\n"
    "v_mfma_f32_16x16x32_bf16 a[76:79], v[180:183], a[244:247], a[76:79]\n"
    "v_mfma_f32_16x16x32_bf16 a[76:79], v[144:147], a[248:251], a[76:79]\n"
    "v_mfma_f32_16x16x32_bf16 a[92:95], v[144:147], a[252:255], a[92:95]\n"
    "v_mfma_f32_16x16x32_bf16 a[92:95], v[180:183], a[248:251], a[92:95]\n"
    "ds_read_b128 a[160:163], v9 offset:28672\n"
    "buffer_load_dwordx4 v3, s[44:47], s59 offen offset:2048 lds\n"
    "v_mfma_f32_16x16x32_bf16 a[92:95], v[84:87], a[244:247], a[92:95]\n"
    "s_waitcnt vmcnt(21)\n"
    "ds_read_b64_tr_b16 v[184:185], v5 offset:20480\n"
    "ds_read_b128 a[164:167], v9 offset:29952\n"
    "v_mfma_f32_16x16x32_bf16 a[108:111], v[84:87], a[248:251], a[108:111]\n"
    "ds_read_b64_tr_b16 v[186:187], v5 offset:20992\n"
    "ds_read_b128 a[136:139], v9 offset:31232\n"
    "v_mfma_f32_16x16x32_bf16 a[108:111], v[180:183], a[252:255], a[108:111]\n"
    "ds_read_b64_tr_b16 v[220:221], v6 offset:20480\n"
    "ds_read_b128 a[172:175], v9 offset:32512\n"
    "v_mfma_f32_16x16x32_bf16 a[108:111], v[144:147], a[128:131], a[108:111]\n"
    "ds_read_b64_tr_b16 v[222:223], v6 offset:20992\n"
    "buffer_load_dwordx4 v4, s[44:47], s59 offen offset:3072 lds\n"
    "v_mfma_f32_16x16x32_bf16 a[124:127], v[144:147], a[132:135], a[124:127]\n"
    "ds_read_b64_tr_b16 a[168:169], v7 offset:20480\n"
    "v_mfma_f32_16x16x32_bf16 a[124:127], v[180:183], a[128:131], a[124:127]\n"
    "ds_read_b64_tr_b16 a[170:171], v7 offset:20992\n"
    "v_mfma_f32_16x16x32_bf16 a[124:127], v[84:87], a[252:255], a[124:127]\n"
    "ds_read_b64_tr_b16 a[152:153], v8 offset:20480\n"
    "s_mov_b32 m0, s72\n"
    "s_waitcnt lgkmcnt(8)\n"
    "v_mfma_f32_16x16x32_bf16 a[0:3], v[116:119], a[160:163], a[0:3]\n"
    "ds_read_b64_tr_b16 a[154:155], v8 offset:20992\n"
    "ds_read_b128 a[176:179], v9 offset:33792\n"
    "v_mfma_f32_16x16x32_bf16 a[0:3], v[208:211], a[164:167], a[0:3]\n"
    "ds_read_b128 a[180:183], v9 offset:35072\n"
    "buffer_load_dwordx4 v1, s[44:47], s60 offen offset:0 lds\n"
    "s_waitcnt lgkmcnt(7)\n"
    "v_mfma_f32_16x16x32_bf16 a[0:3], v[184:187], a[136:139], a[0:3]\n"
    "v_mfma_f32_16x16x32_bf16 a[16:19], v[184:187], a[172:175], a[16:19]\n"
    "v_mfma_f32_16x16x32_bf16 a[16:19], v[208:211], a[136:139], a[16:19]\n"
    "v_mfma_f32_16x16x32_bf16 a[16:19], v[116:119], a[164:167], a[16:19]\n"
    "buffer_load_dwordx4 v2, s[44:47], s60 offen offset:1024 lds\n"
    "v_mfma_f32_16x16x32_bf16 a[32:35], v[116:119], a[136:139], a[32:35]\n"
    "s_waitcnt vmcnt(23)\n"
    "ds_read_b128 a[184:187], v9 offset:36352\n"
    "v_mfma_f32_16x16x32_bf16 a[32:35], v[208:211], a[172:175], a[32:35]\n"
    "s_waitcnt vmcnt(22)\n"
    "ds_read_b128 a[188:191], v9 offset:37632\n"
    "s_waitcnt lgkmcnt(2)\n"
    "v_mfma_f32_16x16x32_bf16 a[32:35], v[184:187], a[176:179], a[32:35]\n"
    "v_mfma_f32_16x16x32_bf16 a[48:51], v[184:187], a[180:183], a[48:51]\n"
    "buffer_load_dwordx4 v3, s[44:47], s60 offen offset:2048 lds\n"
    "v_mfma_f32_16x16x32_bf16 a[48:51], v[208:211], a[176:179], a[48:51]\n"
    "v_mfma_f32_16x16x32_bf16 a[48:51], v[116:119], a[172:175], a[48:51]\n"
    "s_waitcnt vmcnt(19)\n"
    "ds_read_b64_tr_b16 v[224:225], v5 offset:24576\n"
    "v_mfma_f32_16x16x32_bf16 a[64:67], v[116:119], a[176:179], a[64:67]\n"
    "ds_read_b64_tr_b16 v[226:227], v5 offset:25088\n"
    "ds_read_b128 a[192:195], v10 offset:28672\n"
    "v_mfma_f32_16x16x32_bf16 a[64:67], v[208:211], a[180:183], a[64:67]\n"
    "ds_read_b64_tr_b16 v[232:233], v6 offset:24576\n"
    "ds_read_b128 a[196:199], v10 offset:29952\n"
    "s_waitcnt lgkmcnt(5)\n"
    "v_mfma_f32_16x16x32_bf16 a[64:67], v[184:187], a[184:187], a[64:67]\n"
    "ds_read_b64_tr_b16 v[234:235], v6 offset:25088\n"
    "buffer_load_dwordx4 v4, s[44:47], s60 offen offset:3072 lds\n"
    "v_mfma_f32_16x16x32_bf16 a[80:83], v[184:187], a[188:191], a[80:83]\n"
    "ds_read_b64_tr_b16 a[200:201], v7 offset:24576\n"
    "v_mfma_f32_16x16x32_bf16 a[80:83], v[208:211], a[184:187], a[80:83]\n"
    "ds_read_b64_tr_b16 a[202:203], v7 offset:25088\n"
    "v_mfma_f32_16x16x32_bf16 a[80:83], v[116:119], a[180:183], a[80:83]\n"
    "ds_read_b64_tr_b16 a[212:213], v8 offset:24576\n"
    "s_mov_b32 m0, s73\n"
    "v_mfma_f32_16x16x32_bf16 a[96:99], v[116:119], a[184:187], a[96:99]\n"
    "ds_read_b64_tr_b16 a[214:215], v8 offset:25088\n"
    "buffer_load_dwordx4 v1, s[44:47], s61 offen offset:0 lds\n"
    "v_mfma_f32_16x16x32_bf16 a[96:99], v[208:211], a[188:191], a[96:99]\n"
    "s_waitcnt lgkmcnt(5)\n"
    "v_mfma_f32_16x16x32_bf16 a[96:99], v[184:187], a[192:195], a[96:99]\n"
    "v_mfma_f32_16x16x32_bf16 a[112:115], v[184:187], a[196:199], a[112:115]\n"
    "v_mfma_f32_16x16x32_bf16 a[112:115], v[208:211], a[192:195], a[112:115]\n"
    "buffer_load_dwordx4 v2, s[44:47], s61 offen offset:1024 lds\n"
    "v_mfma_f32_16x16x32_bf16 a[112:115], v[116:119], a[188:191], a[112:115]\n"
    "v_mfma_f32_16x16x32_bf16 a[4:7], v[204:207], a[160:163], a[4:7]\n"
    "v_mfma_f32_16x16x32_bf16 a[4:7], v[120:123], a[164:167], a[4:7]\n"
    "v_mfma_f32_16x16x32_bf16 a[4:7], v[220:223], a[136:139], a[4:7]\n"
    "buffer_load_dwordx4 v3, s[44:47], s61 offen offset:2048 lds\n"
    "v_mfma_f32_16x16x32_bf16 a[20:23], v[220:223], a[172:175], a[20:23]\n"
    "s_waitcnt vmcnt(19)\n"
    "ds_read_b64_tr_b16 v[200:201], v5 offset:0\n"
    "v_mfma_f32_16x16x32_bf16 a[20:23], v[120:123], a[136:139], a[20:23]\n"
    "ds_read_b64_tr_b16 v[202:203], v5 offset:512\n"
    "v_mfma_f32_16x16x32_bf16 a[20:23], v[204:207], a[164:167], a[20:23]\n"
    "ds_read_b64_tr_b16 v[252:253], v6 offset:0\n"
    "v_mfma_f32_16x16x32_bf16 a[36:39], v[204:207], a[136:139], a[36:39]\n"
    "ds_read_b64_tr_b16 v[254:255], v6 offset:512\n"
    "buffer_load_dwordx4 v4, s[44:47], s61 offen offset:3072 lds\n"
    "v_mfma_f32_16x16x32_bf16 a[36:39], v[120:123], a[172:175], a[36:39]\n"
    "ds_read_b64_tr_b16 a[208:209], v7 offset:0\n"
    "v_mfma_f32_16x16x32_bf16 a[36:39], v[220:223], a[176:179], a[36:39]\n"
    "ds_read_b64_tr_b16 a[210:211], v7 offset:512\n"
    "v_mfma_f32_16x16x32_bf16 a[52:55], v[220:223], a[180:183], a[52:55]\n"
    "ds_read_b64_tr_b16 a[204:205], v8 offset:0\n"
    "s_mov_b32 m0, s74\n"
    "v_mfma_f32_16x16x32_bf16 a[52:55], v[120:123], a[176:179], a[52:55]\n"
    "ds_read_b64_tr_b16 a[206:207], v8 offset:512\n"
    "s_waitcnt lgkmcnt(6)\n"
    "buffer_load_dwordx4 v1, s[44:47], s62 offen offset:0 lds\n"
    "v_mfma_f32_16x16x32_bf16 a[52:55], v[204:207], a[172:175], a[52:55]\n"
    "v_mfma_f32_16x16x32_bf16 a[68:71], v[204:207], a[176:179], a[68:71]\n"
    "v_mfma_f32_16x16x32_bf16 a[68:71], v[120:123], a[180:183], a[68:71]\n"
    "v_mfma_f32_16x16x32_bf16 a[68:71], v[220:223], a[184:187], a[68:71]\n"
    "buffer_load_dwordx4 v2, s[44:47], s62 offen offset:1024 lds\n"
    "v_mfma_f32_16x16x32_bf16 a[84:87], v[220:223], a[188:191], a[84:87]\n"
    "v_mfma_f32_16x16x32_bf16 a[84:87], v[120:123], a[184:187], a[84:87]\n"
    "v_mfma_f32_16x16x32_bf16 a[84:87], v[204:207], a[180:183], a[84:87]\n"
    "s_waitcnt vmcnt(18)\n"
    "ds_read_b64_tr_b16 v[236:237], v5 offset:4096\n"
    "v_mfma_f32_16x16x32_bf16 a[100:103], v[204:207], a[184:187], a[100:103]\n"
    "ds_read_b64_tr_b16 v[238:239], v5 offset:4608\n"
    "buffer_load_dwordx4 v3, s[44:47], s62 offen offset:2048 lds\n"
    "v_mfma_f32_16x16x32_bf16 a[100:103], v[120:123], a[188:191], a[100:103]\n"
    "L_st5_%=:\n"
    "s_cbranch_execz L_back_%=\n"
    "ds_read_b64_tr_b16 v[228:229], v6 offset:4096\n"
    "v_mfma_f32_16x16x32_bf16 a[100:103], v[220:223], a[192:195], a[100:103]\n"
    "ds_read_b64_tr_b16 v[230:231], v6 offset:4608\n"
    "v_mfma_f32_16x16x32_bf16 a[116:119], v[220:223], a[196:199], a[116:119]\n"
    "ds_read_b64_tr_b16 v[148:149], v7 offset:4096\n"
    "v_mfma_f32_16x16x32_bf16 a[116:119], v[120:123], a[192:195], a[116:119]\n"
    "ds_read_b64_tr_b16 v[150:151], v7 offset:4608\n"
    "buffer_load_dwordx4 v4, s[44:47], s62 offen offset:3072 lds\n"
    "v_mfma_f32_16x16x32_bf16 a[116:119], v[204:207], a[188:191], a[116:119]\n"
    "ds_read_b64_tr_b16 v[152:153], v8 offset:4096\n"
    "v_mfma_f32_16x16x32_bf16 a[0:3], v[224:227], a[172:175], a[0:3]\n"
    "ds_read_b64_tr_b16 v[154:155], v8 offset:4608\n"
    "v_mfma_f32_16x16x32_bf16 a[0:3], v[200:203], a[176:179], a[0:3]\n"
    "s_mov_b32 m0, s68\n"
    "s_waitcnt lgkmcnt(5)\n"
    "v_mfma_f32_16x16x32_bf16 a[0:3], v[236:239], a[180:183], a[0:3]\n"
    "buffer_load_dwordx4 v1, s[44:47], s63 offen offset:0 lds\n"
    "v_mfma_f32_16x16x32_bf16 a[16:19], v[236:239], a[184:187], a[16:19]\n"
    "v_mfma_f32_16x16x32_bf16 a[16:19], v[200:203], a[180:183], a[16:19]\n"
    "v_mfma_f32_16x16x32_bf16 a[16:19], v[224:227], a[176:179], a[16:19]\n"
    "v_mfma_f32_16x16x32_bf16 a[32:35], v[224:227], a[180:183], a[32:35]\n"
    "buffer_load_dwordx4 v2, s[44:47], s63 offen offset:1024 lds\n"
    "v_mfma_f32_16x16x32_bf16 a[32:35], v[200:203], a[184:187], a[32:35]\n"
    "ds_read_b128 a[216:219], v10 offset:31232\n"
    "v_mfma_f32_16x16x32_bf16 a[32:35], v[236:239], a[188:191], a[32:35]\n"
    "v_mfma_f32_16x16x32_bf16 a[48:51], v[236:239], a[192:195], a[48:51]\n"
    "v_mfma_f32_16x16x32_bf16 a[48:51], v[200:203], a[188:191], a[48:51]\n"
    "buffer_load_dwordx4 v3, s[44:47], s63 offen offset:2048 lds\n"
    "v_mfma_f32_16x16x32_bf16 a[48:51], v[224:227], a[184:187], a[48:51]\n"
    "s_waitcnt vmcnt(19)\n"
    "ds_read_b64_tr_b16 v[188:189], v5 offset:8192\n"
    "v_mfma_f32_16x16x32_bf16 a[64:67], v[224:227], a[188:191], a[64:67]\n"
    "ds_read_b64_tr_b16 v[190:191], v5 offset:8704\n"
    "ds_read_b128 a[220:223], v10 offset:32512\n"
    "v_mfma_f32_16x16x32_bf16 a[64:67], v[200:203], a[192:195], a[64:67]\n"
    "ds_read_b64_tr_b16 v[192:193], v6 offset:8192\n"
    "ds_read_b128 a[224:227], v10 offset:33792\n"
    "v_mfma_f32_16x16x32_bf16 a[64:67], v[236:239], a[196:199], a[64:67]\n"
    "ds_read_b64_tr_b16 v[194:195], v6 offset:8704\n"
    "buffer_load_dwordx4 v4, s[44:47], s63 offen offset:3072 lds\n"
    "s_waitcnt lgkmcnt(6)\n"
    "v_mfma_f32_16x16x32_bf16 a[80:83], v[236:239], a[216:219], a[80:83]\n"
    "ds_read_b64_tr_b16 v[164:165], v7 offset:8192\n"
    "v_mfma_f32_16x16x32_bf16 a[80:83], v[200:203], a[196:199], a[80:83]\n"
    "ds_read_b64_tr_b16 v[166:167], v7 offset:8704\n"
    "v_mfma_f32_16x16x32_bf16 a[80:83], v[224:227], a[192:195], a[80:83]\n"
    "ds_read_b64_tr_b16 v[240:241], v8 offset:8192\n"
    "s_mov_b32 m0, s69\n"
    "v_mfma_f32_16x16x32_bf16 a[96:99], v[224:227], a[196:199], a[96:99]\n"
    "ds_read_b64_tr_b16 v[242:243], v8 offset:8704\n"
    "buffer_load_dwordx4 v1, s[44:47], s64 offen offset:0 lds\n"
    "v_mfma_f32_16x16x32_bf16 a[96:99], v[200:203], a[216:219], a[96:99]\n"
    "s_waitcnt lgkmcnt(5)\n"
    "v_mfma_f32_16x16x32_bf16 a[96:99], v[236:239], a[220:223], a[96:99]\n"
    "v_mfma_f32_16x16x32_bf16 a[112:115], v[236:239], a[224:227], a[112:115]\n"
    "v_mfma_f32_16x16x32_bf16 a[112:115], v[200:203], a[220:223], a[112:115]\n"
    "buffer_load_dwordx4 v2, s[44:47], s64 offen offset:1024 lds\n"
    "v_mfma_f32_16x16x32_bf16 a[112:115], v[224:227], a[216:219], a[112:115]\n"
    "v_mfma_f32_16x16x32_bf16 a[4:7], v[232:235], a[172:175], a[4:7]\n"
    "v_mfma_f32_16x16x32_bf16 a[4:7], v[252:255], a[176:179], a[4:7]\n"
    "v_mfma_f32_16x16x32_bf16 a[4:7], v[228:231], a[180:183], a[4:7]\n"
    "buffer_load_dwordx4 v3, s[44:47], s64 offen offset:2048 lds\n"
    "v_mfma_f32_16x16x32_bf16 a[20:23], v[228:231], a[184:187], a[20:23]\n"
    "s_waitcnt vmcnt(19)\n"
    "ds_read_b64_tr_b16 v[244:245], v5 offset:12288\n"
    "v_mfma_f32_16x16x32_bf16 a[20:23], v[252:255], a[180:183], a[20:23]\n"
    "ds_read_b64_tr_b16 v[246:247], v5 offset:12800\n"
    "v_mfma_f32_16x16x32_bf16 a[20:23], v[232:235], a[176:179], a[20:23]\n"
    "ds_read_b64_tr_b16 v[168:169], v6 offset:12288\n"
    "v_mfma_f32_16x16x32_bf16 a[36:39], v[232:235], a[180:183], a[36:39]\n"
    "ds_read_b64_tr_b16 v[170:171], v6 offset:12800\n"
    "buffer_load_dwordx4 v4, s[44:47], s64 offen offset:3072 lds\n"
    "v_mfma_f32_16x16x32_bf16 a[36:39], v[252:255], a[184:187], a[36:39]\n"
    "ds_read_b64_tr_b16 v[216:217], v7 offset:12288\n"
    "v_mfma_f32_16x16x32_bf16 a[36:39], v[228:231], a[188:191], a[36:39]\n"
    "ds_read_b64_tr_b16 v[218:219], v7 offset:12800\n"
    "v_mfma_f32_16x16x32_bf16 a[52:55], v[228:231], a[192:195], a[52:55]\n"
    "ds_read_b64_tr_b16 v[44:45], v8 offset:12288\n"
    "s_mov_b32 m0, s70\n"
    "v_mfma_f32_16x16x32_bf16 a[52:55], v[252:255], a[188:191], a[52:55]\n"
    "ds_read_b64_tr_b16 v[46:47], v8 offset:12800\n"
    "s_waitcnt lgkmcnt(6)\n"
    "buffer_load_dwordx4 v1, s[44:47], s65 offen offset:0 lds\n"
    "v_mfma_f32_16x16x32_bf16 a[52:55], v[232:235], a[184:187], a[52:55]\n"
    "v_mfma_f32_16x16x32_bf16 a[68:71], v[232:235], a[188:191], a[68:71]\n"
    "v_mfma_f32_16x16x32_bf16 a[68:71], v[252:255], a[192:195], a[68:71]\n"
    "v_mfma_f32_16x16x32_bf16 a[68:71], v[228:231], a[196:199], a[68:71]\n"
    "buffer_load_dwordx4 v2, s[44:47], s65 offen offset:1024 lds\n"
    "v_mfma_f32_16x16x32_bf16 a[84:87], v[228:231], a[216:219], a[84:87]\n"
    "v_mfma_f32_16x16x32_bf16 a[84:87], v[252:255], a[196:199], a[84:87]\n"
    "v_mfma_f32_16x16x32_bf16 a[84:87], v[232:235], a[192:195], a[84:87]\n"
    "s_waitcnt vmcnt(18)\n"
    "ds_read_b64_tr_b16 v[48:49], v5 offset:16384\n"
    "v_mfma_f32_16x16x32_bf16 a[100:103], v[232:235], a[196:199], a[100:103]\n"
    "ds_read_b64_tr_b16 v[50:51], v5 offset:16896\n"
    "buffer_load_dwordx4 v3, s[44:47], s65 offen offset:2048 lds\n"
    "v_mfma_f32_16x16x32_bf16 a[100:103], v[252:255], a[216:219], a[100:103]\n"
    "ds_read_b64_tr_b16 v[52:53], v6 offset:16384\n"
    "v_mfma_f32_16x16x32_bf16 a[100:103], v[228:231], a[220:223], a[100:103]\n"
    "ds_read_b64_tr_b16 v[54:55], v6 offset:16896\n"
    "v_mfma_f32_16x16x32_bf16 a[116:119], v[228:231], a[224:227], a[116:119]\n"
    "ds_read_b64_tr_b16 v[196:197], v7 offset:16384\n"
    "v_mfma_f32_16x16x32_bf16 a[116:119], v[252:255], a[220:223], a[116:119]\n"
    "ds_read_b64_tr_b16 v[198:199], v7 offset:16896\n"
    "buffer_load_dwordx4 v4, s[44:47], s65 offen offset:3072 lds\n"
    "v_mfma_f32_16x16x32_bf16 a[116:119], v[232:235], a[216:219], a[116:119]\n"
    "ds_read_b64_tr_b16 v[60:61], v8 offset:16384\n"
    "v_mfma_f32_16x16x32_bf16 a[0:3], v[188:191], a[184:187], a[0:3]\n"
    "ds_read_b64_tr_b16 v[62:63], v8 offset:16896\n"
    "v_mfma_f32_16x16x32_bf16 a[0:3], v[244:247], a[188:191], a[0:3]\n"
    "s_mov_b32 m0, s71\n"
    "s_waitcnt lgkmcnt(5)\n"
    "v_mfma_f32_16x16x32_bf16 a[0:3], v[48:51], a[192:195], a[0:3]\n"
    "buffer_load_dwordx4 v1, s[44:47], s66 offen offset:0 lds\n"
    "v_mfma_f32_16x16x32_bf16 a[16:19], v[48:51], a[196:199], a[16:19]\n"
    "v_mfma_f32_16x16x32_bf16 a[16:19], v[244:247], a[192:195], a[16:19]\n"
    "v_mfma_f32_16x16x32_bf16 a[16:19], v[188:191], a[188:191], a[16:19]\n"
    "v_mfma_f32_16x16x32_bf16 a[32:35], v[188:191], a[192:195], a[32:35]\n"
    "buffer_load_dwordx4 v2, s[44:47], s66 offen offset:1024 lds\n"
    "v_mfma_f32_16x16x32_bf16 a[32:35], v[244:247], a[196:199], a[32:35]\n"
    "ds_read_b128 a[228:231], v10 offset:35072\n"
    "v_mfma_f32_16x16x32_bf16 a[32:35], v[48:51], a[216:219], a[32:35]\n"
    "s_waitcnt vmcnt(18)\n"
    "ds_read_b64_tr_b16 v[64:65], v5 offset:20480\n"
    "v_mfma_f32_16x16x32_bf16 a[48:51], v[48:51], a[220:223], a[48:51]\n"
    "ds_read_b64_tr_b16 v[66:67], v5 offset:20992\n"
    "v_mfma_f32_16x16x32_bf16 a[48:51], v[244:247], a[216:219], a[48:51]\n"
    "ds_read_b64_tr_b16 v[68:69], v6 offset:20480\n"
    "buffer_load_dwordx4 v3, s[44:47], s66 offen offset:2048 lds\n"
    "v_mfma_f32_16x16x32_bf16 a[48:51], v[188:191], a[196:199], a[48:51]\n"
    "ds_read_b64_tr_b16 v[70:71], v6 offset:20992\n"
    "v_mfma_f32_16x16x32_bf16 a[64:67], v[188:191], a[216:219], a[64:67]\n"
    "ds_read_b64_tr_b16 v[212:213], v7 offset:20480\n"
    "ds_read_b128 a[232:235], v10 offset:36352\n"
    "v_mfma_f32_16x16x32_bf16 a[64:67], v[244:247], a[220:223], a[64:67]\n"
    "ds_read_b64_tr_b16 v[214:215], v7 offset:20992\n"
    "ds_read_b128 a[236:239], v10 offset:37632\n"
    "v_mfma_f32_16x16x32_bf16 a[64:67], v[48:51], a[224:227], a[64:67]\n"
    "ds_read_b64_tr_b16 v[76:77], v8 offset:20480\n"
    "buffer_load_dwordx4 v4, s[44:47], s66 offen offset:3072 lds\n"
    "s_waitcnt lgkmcnt(8)\n"
    "v_mfma_f32_16x16x32_bf16 a[80:83], v[48:51], a[228:231], a[80:83]\n"
    "ds_read_b64_tr_b16 v[78:79], v8 offset:20992\n"
    "v_mfma_f32_16x16x32_bf16 a[80:83], v[244:247], a[224:227], a[80:83]\n"
    "v_mfma_f32_16x16x32_bf16 a[80:83], v[188:191], a[220:223], a[80:83]\n"
    "s_waitcnt vmcnt(16)\n"
    "ds_read_b64_tr_b16 v[80:81], v5 offset:24576\n"
    "v_mfma_f32_16x16x32_bf16 a[96:99], v[188:191], a[224:227], a[96:99]\n"
    "ds_read_b64_tr_b16 v[82:83], v5 offset:25088\n"
    "v_mfma_f32_16x16x32_bf16 a[96:99], v[244:247], a[228:231], a[96:99]\n"
    "ds_read_b64_tr_b16 v[56:57], v6 offset:24576\n"
    "s_waitcnt lgkmcnt(5)\n"
    "v_mfma_f32_16x16x32_bf16 a[96:99], v[48:51], a[232:235], a[96:99]\n"
    "ds_read_b64_tr_b16 v[58:59], v6 offset:25088\n"
    "global_load_dword v27, v25, s[82:83] sc1\n"
    "v_mfma_f32_16x16x32_bf16 a[112:115], v[48:51], a[236:239], a[112:115]\n"
    "ds_read_b64_tr_b16 v[88:89], v7 offset:24576\n"
    "v_mfma_f32_16x16x32_bf16 a[112:115], v[244:247], a[232:235], a[112:115]\n"
    "ds_read_b64_tr_b16 v[90:91], v7 offset:25088\n"
    "v_mfma_f32_16x16x32_bf16 a[112:115], v[188:191], a[228:231], a[112:115]\n"
    "ds_read_b64_tr_b16 v[124:125], v8 offset:24576\n"
    "v_mfma_f32_16x16x32_bf16 a[4:7], v[192:195], a[184:187], a[4:7]\n"
    "ds_read_b64_tr_b16 v[126:127], v8 offset:25088\n"
    "v_mfma_f32_16x16x32_bf16 a[4:7], v[168:171], a[188:191], a[4:7]\n"
    "v_mfma_f32_16x16x32_bf16 a[4:7], v[52:55], a[192:195], a[4:7]\n"
    "s_waitcnt vmcnt(13)\n"
    "ds_read_b64_tr_b16 v[108:109], v5 offset:0\n"
    "v_mfma_f32_16x16x32_bf16 a[20:23], v[52:55], a[196:199], a[20:23]\n"
    "ds_read_b64_tr_b16 v[110:111], v5 offset:512\n"
    "v_mfma_f32_16x16x32_bf16 a[20:23], v[168:171], a[192:195], a[20:23]\n"
    "ds_read_b64_tr_b16 v[92:93], v6 offset:0\n"
    "v_mfma_f32_16x16x32_bf16 a[20:23], v[192:195], a[188:191], a[20:23]\n"
    "ds_read_b64_tr_b16 v[94:95], v6 offset:512\n"
    "v_mfma_f32_16x16x32_bf16 a[36:39], v[192:195], a[192:195], a[36:39]\n"
    "ds_read_b64_tr_b16 a[240:241], v7 offset:0\n"
    "v_mfma_f32_16x16x32_bf16 a[36:39], v[168:171], a[196:199], a[36:39]\n"
    "ds_read_b64_tr_b16 a[242:243], v7 offset:512\n"
    "v_mfma_f32_16x16x32_bf16 a[36:39], v[52:55], a[216:219], a[36:39]\n"
    "ds_read_b64_tr_b16 a[244:245], v8 offset:0\n"
    "v_mfma_f32_16x16x32_bf16 a[52:55], v[52:55], a[220:223], a[52:55]\n"
    "ds_read_b64_tr_b16 a[246:247], v8 offset:512\n"
    "v_mfma_f32_16x16x32_bf16 a[52:55], v[168:171], a[216:219], a[52:55]\n"
    "v_mfma_f32_16x16x32_bf16 a[52:55], v[192:195], a[196:199], a[52:55]\n"
    "s_waitcnt vmcnt(9)\n"
    "ds_read_b64_tr_b16 v[128:129], v5 offset:4096\n"
    "v_mfma_f32_16x16x32_bf16 a[68:71], v[192:195], a[216:219], a[68:71]\n"
    "ds_read_b64_tr_b16 v[130:131], v5 offset:4608\n"
    "v_mfma_f32_16x16x32_bf16 a[68:71], v[168:171], a[220:223], a[68:71]\n"
    "ds_read_b64_tr_b16 v[112:113], v6 offset:4096\n"
    "v_mfma_f32_16x16x32_bf16 a[68:71], v[52:55], a[224:227], a[68:71]\n"
    "ds_read_b64_tr_b16 v[114:115], v6 offset:4608\n"
    "v_mfma_f32_16x16x32_bf16 a[84:87], v[52:55], a[228:231], a[84:87]\n"
    "ds_read_b64_tr_b16 a[248:249], v7 offset:4096\n"
    "v_mfma_f32_16x16x32_bf16 a[84:87], v[168:171], a[224:227], a[84:87]\n"
    "ds_read_b64_tr_b16 a[250:251], v7 offset:4608\n"
    "v_mfma_f32_16x16x32_bf16 a[84:87], v[192:195], a[220:223], a[84:87]\n"
    "ds_read_b64_tr_b16 a[132:133], v8 offset:4096\n"
    "v_mfma_f32_16x16x32_bf16 a[100:103], v[192:195], a[224:227], a[100:103]\n"
    "ds_read_b64_tr_b16 a[134:135], v8 offset:4608\n"
    "v_mfma_f32_16x16x32_bf16 a[100:103], v[168:171], a[228:231], a[100:103]\n"
    "v_mfma_f32_16x16x32_bf16 a[100:103], v[52:55], a[232:235], a[100:103]\n"
    "s_waitcnt vmcnt(5)\n"
    "ds_read_b64_tr_b16 v[96:97], v5 offset:8192\n"
    "v_mfma_f32_16x16x32_bf16 a[116:119], v[52:55], a[236:239], a[116:119]\n"
    "ds_read_b64_tr_b16 v[98:99], v5 offset:8704\n"
    "v_mfma_f32_16x16x32_bf16 a[116:119], v[168:171], a[232:235], a[116:119]\n"
    "ds_read_b64_tr_b16 v[132:133], v6 offset:8192\n"
    "v_mfma_f32_16x16x32_bf16 a[116:119], v[192:195], a[228:231], a[116:119]\n"
    "ds_read_b64_tr_b16 v[134:135], v6 offset:8704\n"
    "v_mfma_f32_16x16x32_bf16 a[0:3], v[64:67], a[196:199], a[0:3]\n"
    "ds_read_b64_tr_b16 a[128:129], v7 offset:8192\n"
    "s_waitcnt lgkmcnt(5)\n"
    "v_mfma_f32_16x16x32_bf16 a[0:3], v[80:83], a[216:219], a[0:3]\n"
    "ds_read_b64_tr_b16 a[130:131], v7 offset:8704\n"
    "v_mfma_f32_16x16x32_bf16 a[0:3], v[108:111], a[220:223], a[0:3]\n"
    "ds_read_b64_tr_b16 a[252:253], v8 offset:8192\n"
    "v_mfma_f32_16x16x32_bf16 a[16:19], v[108:111], a[224:227], a[16:19]\n"
    "ds_read_b64_tr_b16 a[254:255], v8 offset:8704\n"
    "v_mfma_f32_16x16x32_bf16 a[16:19], v[80:83], a[220:223], a[16:19]\n"
    "v_mfma_f32_16x16x32_bf16 a[16:19], v[64:67], a[216:219], a[16:19]\n"
    "s_waitcnt vmcnt(1)\n"
    "ds_read_b64_tr_b16 v[248:249], v5 offset:12288\n"
    "v_mfma_f32_16x16x32_bf16 a[4:7], v[68:71], a[196:199], a[4:7]\n"
    "ds_read_b64_tr_b16 v[250:251], v5 offset:12800\n"
    "v_mfma_f32_16x16x32_bf16 a[4:7], v[56:59], a[216:219], a[4:7]\n"
    "ds_read_b64_tr_b16 v[100:101], v6 offset:12288\n"
    "v_mfma_f32_16x16x32_bf16 a[4:7], v[92:95], a[220:223], a[4:7]\n"
    "ds_read_b64_tr_b16 v[102:103], v6 offset:12800\n"
    "v_mfma_f32_16x16x32_bf16 a[20:23], v[92:95], a[224:227], a[20:23]\n"
    "ds_read_b64_tr_b16 v[136:137], v7 offset:12288\n"
    "v_mfma_f32_16x16x32_bf16 a[20:23], v[56:59], a[220:223], a[20:23]\n"
    "ds_read_b64_tr_b16 v[138:139], v7 offset:12800\n"
    "v_mfma_f32_16x16x32_bf16 a[20:23], v[68:71], a[216:219], a[20:23]\n"
    "ds_read_b64_tr_b16 v[72:73], v8 offset:12288\n"
    "v_mfma_f32_16x16x32_bf16 a[0:3], v[128:131], a[224:227], a[0:3]\n"
    "ds_read_b64_tr_b16 v[74:75], v8 offset:12800\n"
    "s_waitcnt lgkmcnt(6)\n"
    "v_mfma_f32_16x16x32_bf16 a[0:3], v[96:99], a[228:231], a[0:3]\n"
    "v_mfma_f32_16x16x32_bf16 a[0:3], v[248:251], a[232:235], a[0:3]\n"
    "v_mfma_f32_16x16x32_bf16 a[16:19], v[248:251], a[236:239], a[16:19]\n"
    "v_mfma_f32_16x16x32_bf16 a[16:19], v[96:99], a[232:235], a[16:19]\n"
    "v_mfma_f32_16x16x32_bf16 a[16:19], v[128:131], a[228:231], a[16:19]\n"
    "ds_write_b128 v28, a[0:3] offset:0\n"
    "v_mfma_f32_16x16x32_bf16 a[4:7], v[112:115], a[224:227], a[4:7]\n"
    "v_mfma_f32_16x16x32_bf16 a[4:7], v[132:135], a[228:231], a[4:7]\n"
    "s_waitcnt lgkmcnt(1)\n"
    "v_mfma_f32_16x16x32_bf16 a[4:7], v[100:103], a[232:235], a[4:7]\n"
    "ds_write_b128 v28, a[16:19] offset:2048\n"
    "v_mfma_f32_16x16x32_bf16 a[20:23], v[100:103], a[236:239], a[20:23]\n"
    "v_mfma_f32_16x16x32_bf16 a[20:23], v[132:135], a[232:235], a[20:23]\n"
    "v_mfma_f32_16x16x32_bf16 a[20:23], v[112:115], a[228:231], a[20:23]\n"
    "ds_write_b128 v28, a[4:7] offset:1024\n"
    "v_mfma_f32_16x16x32_bf16 a[32:35], v[64:67], a[220:223], a[32:35]\n"
    "v_mfma_f32_16x16x32_bf16 a[32:35], v[80:83], a[224:227], a[32:35]\n"
    "v_mfma_f32_16x16x32_bf16 a[32:35], v[108:111], a[228:231], a[32:35]\n"
    "ds_write_b128 v28, a[20:23] offset:3072\n"
    "v_mfma_f32_16x16x32_bf16 a[48:51], v[108:111], a[232:235], a[48:51]\n"
    "v_mfma_f32_16x16x32_bf16 a[48:51], v[80:83], a[228:231], a[48:51]\n"
    "v_mfma_f32_16x16x32_bf16 a[48:51], v[64:67], a[224:227], a[48:51]\n"
    "v_mfma_f32_16x16x32_bf16 a[36:39], v[68:71], a[220:223], a[36:39]\n"
    "ds_read_b128 v[104:107], v11 offset:28928\n"
    "v_mfma_f32_16x16x32_bf16 a[36:39], v[56:59], a[224:227], a[36:39]\n"
    "ds_read_b128 v[172:175], v11 offset:30208\n"
    "v_mfma_f32_16x16x32_bf16 a[36:39], v[92:95], a[228:231], a[36:39]\n"
    "v_mfma_f32_16x16x32_bf16 a[52:55], v[92:95], a[232:235], a[52:55]\n"
    "s_waitcnt lgkmcnt(2)\n"
    "s_barrier\n"
    "v_mfma_f32_16x16x32_bf16 a[52:55], v[56:59], a[228:231], a[52:55]\n"
    "ds_read_b128 v[156:159], v29 offset:0\n"
    "ds_read_b128 v[140:143], v30 offset:0\n"
    "v_mfma_f32_16x16x32_bf16 a[52:55], v[68:71], a[224:227], a[52:55]\n"
    "ds_read_b128 v[176:179], v31 offset:0\n"
    "ds_read_b128 v[160:163], v32 offset:0\n"
    "v_mfma_f32_16x16x32_bf16 a[32:35], v[128:131], a[232:235], a[32:35]\n"
    "v_mfma_f32_16x16x32_bf16 a[32:35], v[96:99], a[236:239], a[32:35]\n"
    "s_waitcnt lgkmcnt(4)\n"
    "v_mfma_f32_16x16x32_bf16 a[32:35], v[248:251], v[104:107], a[32:35]\n"
    "v_mfma_f32_16x16x32_bf16 a[48:51], v[248:251], v[172:175], a[48:51]\n"
    "v_mfma_f32_16x16x32_bf16 a[48:51], v[96:99], v[104:107], a[48:51]\n"
    "v_mfma_f32_16x16x32_bf16 a[48:51], v[128:131], a[236:239], a[48:51]\n"
    "ds_write_b128 v28, a[32:35] offset:4096\n"
    "v_mfma_f32_16x16x32_bf16 a[36:39], v[112:115], a[232:235], a[36:39]\n"
    "v_mfma_f32_16x16x32_bf16 a[36:39], v[132:135], a[236:239], a[36:39]\n"
    "s_waitcnt lgkmcnt(1)\n"
    "v_add_f32 v156, v156, v140\n"
    "v_add_f32 v157, v157, v141\n"
    "v_mfma_f32_16x16x32_bf16 a[36:39], v[100:103], v[104:107], a[36:39]\n"
    "ds_write_b128 v28, a[48:51] offset:6144\n"
    "v_mfma_f32_16x16x32_bf16 a[52:55], v[100:103], v[172:175], a[52:55]\n"
    "v_add_f32 v158, v158, v142\n"
    "v_add_f32 v159, v159, v143\n"
    "v_mfma_f32_16x16x32_bf16 a[52:55], v[132:135], v[104:107], a[52:55]\n"
    "v_add_f32 v176, v176, v160\n"
    "v_add_f32 v177, v177, v161\n"
    "v_mfma_f32_16x16x32_bf16 a[52:55], v[112:115], a[236:239], a[52:55]\n"
    "ds_write_b128 v28, a[36:39] offset:5120\n"
    "v_mfma_f32_16x16x32_bf16 a[64:67], v[64:67], a[228:231], a[64:67]\n"
    "v_add_f32 v178, v178, v162\n"
    "v_add_f32 v179, v179, v163\n"
    "v_mfma_f32_16x16x32_bf16 a[64:67], v[80:83], a[232:235], a[64:67]\n"
    "v_mul_f32 v156, s84, v156\n"
    "v_mul_f32 v157, s84, v157\n"
    "v_mfma_f32_16x16x32_bf16 a[64:67], v[108:111], a[236:239], a[64:67]\n"
    "ds_write_b128 v28, a[52:55] offset:7168\n"
    "v_mfma_f32_16x16x32_bf16 a[80:83], v[108:111], v[104:107], a[80:83]\n"
    "v_mul_f32 v158, s84, v158\n"
    "v_mul_f32 v159, s84, v159\n"
    "v_mfma_f32_16x16x32_bf16 a[80:83], v[80:83], a[236:239], a[80:83]\n"
    "v_fma_f32 v156, v176, s84, v156\n"
    "v_fma_f32 v157, v177, s84, v157\n"
    "v_mfma_f32_16x16x32_bf16 a[80:83], v[64:67], a[232:235], a[80:83]\n"
    "v_fma_f32 v158, v178, s84, v158\n"
    "v_fma_f32 v159, v179, s84, v159\n"
    "v_mfma_f32_16x16x32_bf16 a[68:71], v[68:71], a[228:231], a[68:71]\n"
    "ds_read_b128 v[144:147], v11 offset:31488\n"
    "v_mfma_f32_16x16x32_bf16 a[68:71], v[56:59], a[232:235], a[68:71]\n"
    "ds_read_b128 v[180:183], v11 offset:32768\n"
    "v_mfma_f32_16x16x32_bf16 a[68:71], v[92:95], a[236:239], a[68:71]\n"
    "v_exp_f32 v156, v156\n"
    "v_mfma_f32_16x16x32_bf16 a[84:87], v[92:95], v[104:107], a[84:87]\n"
    "s_waitcnt lgkmcnt(2)\n"
    "s_barrier\n"
    "v_mfma_f32_16x16x32_bf16 a[84:87], v[56:59], a[236:239], a[84:87]\n"
    "ds_read_b128 v[84:87], v29 offset:4096\n"
    "ds_read_b128 v[184:187], v30 offset:4096\n"
    "v_mfma_f32_16x16x32_bf16 a[84:87], v[68:71], a[232:235], a[84:87]\n"
    "ds_read_b128 v[208:211], v31 offset:4096\n"
    "ds_read_b128 v[116:119], v32 offset:4096\n"
    "v_mfma_f32_16x16x32_bf16 a[64:67], v[128:131], v[104:107], a[64:67]\n"
    "v_exp_f32 v157, v157\n"
    "v_mfma_f32_16x16x32_bf16 a[64:67], v[96:99], v[172:175], a[64:67]\n"
    "v_exp_f32 v158, v158\n"
    "s_waitcnt lgkmcnt(4)\n"
    "v_mfma_f32_16x16x32_bf16 a[64:67], v[248:251], v[144:147], a[64:67]\n"
    "v_exp_f32 v159, v159\n"
    "v_mfma_f32_16x16x32_bf16 a[80:83], v[248:251], v[180:183], a[80:83]\n"
    "v_add_f32 v156, 1.0, v156\n"
    "v_add_f32 v157, 1.0, v157\n"
    "v_mfma_f32_16x16x32_bf16 a[80:83], v[96:99], v[144:147], a[80:83]\n"
    "v_add_f32 v158, 1.0, v158\n"
    "v_add_f32 v159, 1.0, v159\n"
    "v_mfma_f32_16x16x32_bf16 a[80:83], v[128:131], v[172:175], a[80:83]\n"
    "ds_write_b128 v28, a[64:67] offset:8192\n"
    "v_mfma_f32_16x16x32_bf16 a[68:71], v[112:115], v[104:107], a[68:71]\n"
    "v_rcp_f32 v156, v156\n"
    "v_mfma_f32_16x16x32_bf16 a[68:71], v[132:135], v[172:175], a[68:71]\n"
    "s_waitcnt vmcnt(0)\n"
    "v_and_b32 v27, 31, v27\n"
    "v_lshl_or_b32 v0, v26, 8, v27\n"
    "v_mfma_f32_16x16x32_bf16 a[68:71], v[100:103], v[144:147], a[68:71]\n"
    "v_lshl_or_b32 v35, v27, 8, v26\n"
    "v_mov_b32 v221, v0\n"
    "v_mfma_f32_16x16x32_bf16 a[84:87], v[100:103], v[180:183], a[84:87]\n"
    "v_mov_b32 v223, v0\n"
    "v_mov_b32 v121, v0\n"
    "v_mfma_f32_16x16x32_bf16 a[84:87], v[132:135], v[144:147], a[84:87]\n"
    "v_mov_b32 v123, v0\n"
    "v_mov_b32 v205, v0\n"
    "v_mfma_f32_16x16x32_bf16 a[84:87], v[112:115], v[172:175], a[84:87]\n"
    "v_mov_b32 v207, v0\n"
    "v_mov_b32 v237, v0\n"
    "v_mfma_f32_16x16x32_bf16 a[96:99], v[64:67], a[236:239], a[96:99]\n"
    "v_mov_b32 v239, v0\n"
    "v_add_u32 v37, 4096, v33\n"
    "v_mfma_f32_16x16x32_bf16 a[96:99], v[80:83], v[104:107], a[96:99]\n"
    "v_add_u32 v38, 8192, v33\n"
    "v_add_u32 v39, 12288, v33\n"
    "v_mfma_f32_16x16x32_bf16 a[96:99], v[108:111], v[172:175], a[96:99]\n"
    "ds_write_b128 v28, a[80:83] offset:10240\n"
    "v_mfma_f32_16x16x32_bf16 a[112:115], v[108:111], v[144:147], a[112:115]\n"
    "ds_write_b128 v28, a[68:71] offset:9216\n"
    "v_mfma_f32_16x16x32_bf16 a[112:115], v[80:83], v[172:175], a[112:115]\n"
    "ds_write_b128 v28, a[84:87] offset:11264\n"
    "v_mfma_f32_16x16x32_bf16 a[112:115], v[64:67], v[104:107], a[112:115]\n"
    "v_rcp_f32 v157, v157\n"
    "v_mfma_f32_16x16x32_bf16 a[100:103], v[68:71], a[236:239], a[100:103]\n"
    "ds_read_b128 v[200:203], v11 offset:34048\n"
    "s_waitcnt lgkmcnt(4)\n"
    "v_add_f32 v84, v84, v184\n"
    "v_mfma_f32_16x16x32_bf16 a[100:103], v[56:59], v[104:107], a[100:103]\n"
    "ds_read_b128 v[224:227], v11 offset:35328\n"
    "v_add_f32 v85, v85, v185\n"
    "v_mfma_f32_16x16x32_bf16 a[100:103], v[92:95], v[172:175], a[100:103]\n"
    "v_rcp_f32 v158, v158\n"
    "v_mfma_f32_16x16x32_bf16 a[116:119], v[92:95], v[144:147], a[116:119]\n"
    "v_rcp_f32 v159, v159\n"
    "v_mfma_f32_16x16x32_bf16 a[116:119], v[56:59], v[172:175], a[116:119]\n"
    "v_fma_f32 v156, v156, s85, v34\n"
    "v_fma_f32 v157, v157, s85, v34\n"
    "v_mfma_f32_16x16x32_bf16 a[116:119], v[68:71], v[104:107], a[116:119]\n"
    "s_waitcnt lgkmcnt(2)\n"
    "s_barrier\n"
    "v_mfma_f32_16x16x32_bf16 a[96:99], v[128:131], v[144:147], a[96:99]\n"
    "ds_read_b128 v[228:231], v29 offset:8192\n"
    "ds_read_b128 v[252:255], v30 offset:8192\n"
    "v_mfma_f32_16x16x32_bf16 a[96:99], v[96:99], v[180:183], a[96:99]\n"
    "ds_read_b128 v[232:235], v31 offset:8192\n"
    "ds_read_b128 v[48:51], v32 offset:8192\n"
    "s_waitcnt lgkmcnt(4)\n"
    "v_mfma_f32_16x16x32_bf16 a[96:99], v[248:251], v[200:203], a[96:99]\n"
    "v_fma_f32 v158, v158, s85, v34\n"
    "v_fma_f32 v159, v159, s85, v34\n"
    "v_mfma_f32_16x16x32_bf16 a[112:115], v[248:251], v[224:227], a[112:115]\n"
    "v_cvt_pk_bf16_f32 v220, v156, v157\n"
    "v_cvt_pk_bf16_f32 v222, v158, v159\n"
    "v_mfma_f32_16x16x32_bf16 a[112:115], v[96:99], v[200:203], a[112:115]\n"
    "global_store_dwordx4 v24, v[220:223], s[78:79] offset:0 sc1\n"
    "v_add_f32 v86, v86, v186\n"
    "v_mfma_f32_16x16x32_bf16 a[112:115], v[128:131], v[180:183], a[112:115]\n"
    "ds_write_b128 v28, a[96:99] offset:12288\n"
    "v_mfma_f32_16x16x32_bf16 a[100:103], v[112:115], v[144:147], a[100:103]\n"
    "v_add_f32 v87, v87, v187\n"
    "v_add_f32 v208, v208, v116\n"
    "v_mfma_f32_16x16x32_bf16 a[100:103], v[132:135], v[180:183], a[100:103]\n"
    "v_add_f32 v209, v209, v117\n"
    "v_add_f32 v210, v210, v118\n"
    "v_mfma_f32_16x16x32_bf16 a[100:103], v[100:103], v[200:203], a[100:103]\n"
    "ds_write_b128 v28, a[112:115] offset:14336\n"
    "v_mfma_f32_16x16x32_bf16 a[116:119], v[100:103], v[224:227], a[116:119]\n"
    "v_add_f32 v211, v211, v119\n"
    "v_mul_f32 v84, s84, v84\n"
    "v_mfma_f32_16x16x32_bf16 a[116:119], v[132:135], v[200:203], a[116:119]\n"
    "v_mul_f32 v85, s84, v85\n"
    "v_mul_f32 v86, s84, v86\n"
    "v_mfma_f32_16x16x32_bf16 a[116:119], v[112:115], v[180:183], a[116:119]\n"
    "ds_write_b128 v28, a[100:103] offset:13312\n"
    "v_mfma_f32_16x16x32_bf16 a[8:11], a[140:143], a[160:163], a[8:11]\n"
    "v_mul_f32 v87, s84, v87\n"
    "v_fma_f32 v84, v208, s84, v84\n"
    "v_mfma_f32_16x16x32_bf16 a[8:11], a[148:151], a[164:167], a[8:11]\n"
    "v_fma_f32 v85, v209, s84, v85\n"
    "v_fma_f32 v86, v210, s84, v86\n"
    "v_mfma_f32_16x16x32_bf16 a[8:11], a[168:171], a[136:139], a[8:11]\n"
    "ds_write_b128 v28, a[116:119] offset:15360\n"
    "v_mfma_f32_16x16x32_bf16 a[24:27], a[168:171], a[172:175], a[24:27]\n"
    "v_fma_f32 v87, v211, s84, v87\n"
    "s_waitcnt lgkmcnt(2)\n"
    "v_add_f32 v228, v228, v252\n"
    "v_mfma_f32_16x16x32_bf16 a[24:27], a[148:151], a[136:139], a[24:27]\n"
    "v_exp_f32 v84, v84\n"
    "v_mfma_f32_16x16x32_bf16 a[24:27], a[140:143], a[164:167], a[24:27]\n"
    "v_exp_f32 v85, v85\n"
    "v_mfma_f32_16x16x32_bf16 a[12:15], a[144:147], a[160:163], a[12:15]\n"
    "v_exp_f32 v86, v86\n"
    "v_mfma_f32_16x16x32_bf16 a[12:15], a[156:159], a[164:167], a[12:15]\n"
    "v_exp_f32 v87, v87\n"
    "v_mfma_f32_16x16x32_bf16 a[12:15], a[152:155], a[136:139], a[12:15]\n"
    "v_add_f32 v84, 1.0, v84\n"
    "v_add_f32 v85, 1.0, v85\n"
    "v_mfma_f32_16x16x32_bf16 a[28:31], a[152:155], a[172:175], a[28:31]\n"
    "s_waitcnt lgkmcnt(0)\n"
    "s_barrier\n"
    "v_mfma_f32_16x16x32_bf16 a[28:31], a[156:159], a[136:139], a[28:31]\n"
    "ds_read_b128 v[244:247], v29 offset:12288\n"
    "ds_read_b128 v[188:191], v30 offset:12288\n"
    "v_mfma_f32_16x16x32_bf16 a[28:31], a[144:147], a[164:167], a[28:31]\n"
    "ds_read_b128 v[52:55], v31 offset:12288\n"
    "ds_read_b128 v[168:171], v32 offset:12288\n"
    "v_mfma_f32_16x16x32_bf16 a[8:11], a[200:203], a[172:175], a[8:11]\n"
    "v_add_f32 v86, 1.0, v86\n"
    "v_add_f32 v87, 1.0, v87\n"
    "v_mfma_f32_16x16x32_bf16 a[8:11], a[208:211], a[176:179], a[8:11]\n"
    "v_rcp_f32 v84, v84\n"
    "v_mfma_f32_16x16x32_bf16 a[8:11], v[148:151], a[180:183], a[8:11]\n"
    "v_rcp_f32 v85, v85\n"
    "v_mfma_f32_16x16x32_bf16 a[24:27], v[148:151], a[184:187], a[24:27]\n"
    "v_rcp_f32 v86, v86\n"
    "v_mfma_f32_16x16x32_bf16 a[24:27], a[208:211], a[180:183], a[24:27]\n"
    "v_rcp_f32 v87, v87\n"
    "v_mfma_f32_16x16x32_bf16 a[24:27], a[200:203], a[176:179], a[24:27]\n"
    "v_fma_f32 v84, v84, s85, v34\n"
    "v_fma_f32 v85, v85, s85, v34\n"
    "v_mfma_f32_16x16x32_bf16 a[12:15], a[212:215], a[172:175], a[12:15]\n"
    "v_fma_f32 v86, v86, s85, v34\n"
    "v_fma_f32 v87, v87, s85, v34\n"
    "v_mfma_f32_16x16x32_bf16 a[12:15], a[204:207], a[176:179], a[12:15]\n"
    "v_cvt_pk_bf16_f32 v120, v84, v85\n"
    "v_cvt_pk_bf16_f32 v122, v86, v87\n"
    "v_mfma_f32_16x16x32_bf16 a[12:15], v[152:155], a[180:183], a[12:15]\n"
    "global_store_dwordx4 v24, v[120:123], s[78:79] offset:1024 sc1\n"
    "v_add_f32 v229, v229, v253\n"
    "v_mfma_f32_16x16x32_bf16 a[28:31], v[152:155], a[184:187], a[28:31]\n"
    "v_add_f32 v230, v230, v254\n"
    "v_add_f32 v231, v231, v255\n"
    "v_mfma_f32_16x16x32_bf16 a[28:31], a[204:207], a[180:183], a[28:31]\n"
    "v_add_f32 v232, v232, v48\n"
    "v_add_f32 v233, v233, v49\n"
    "v_mfma_f32_16x16x32_bf16 a[28:31], a[212:215], a[176:179], a[28:31]\n"
    "v_add_f32 v234, v234, v50\n"
    "v_add_f32 v235, v235, v51\n"
    "v_mfma_f32_16x16x32_bf16 a[8:11], v[164:167], a[184:187], a[8:11]\n"
    "v_mul_f32 v228, s84, v228\n"
    "v_mul_f32 v229, s84, v229\n"
    "v_mfma_f32_16x16x32_bf16 a[8:11], v[216:219], a[188:191], a[8:11]\n"
    "v_mul_f32 v230, s84, v230\n"
    "v_mul_f32 v231, s84, v231\n"
    "v_mfma_f32_16x16x32_bf16 a[8:11], v[196:199], a[192:195], a[8:11]\n"
    "v_fma_f32 v228, v232, s84, v228\n"
    "v_fma_f32 v229, v233, s84, v229\n"
    "v_mfma_f32_16x16x32_bf16 a[24:27], v[196:199], a[196:199], a[24:27]\n"
    "v_fma_f32 v230, v234, s84, v230\n"
    "v_fma_f32 v231, v235, s84, v231\n"
    "v_mfma_f32_16x16x32_bf16 a[24:27], v[216:219], a[192:195], a[24:27]\n"
    "v_exp_f32 v228, v228\n"
    "v_mfma_f32_16x16x32_bf16 a[24:27], v[164:167], a[188:191], a[24:27]\n"
    "v_exp_f32 v229, v229\n"
    "v_mfma_f32_16x16x32_bf16 a[12:15], v[240:243], a[184:187], a[12:15]\n"
    "v_exp_f32 v230, v230\n"
    "v_mfma_f32_16x16x32_bf16 a[12:15], v[44:47], a[188:191], a[12:15]\n"
    "v_exp_f32 v231, v231\n"
    "v_mfma_f32_16x16x32_bf16 a[12:15], v[60:63], a[192:195], a[12:15]\n"
    "v_add_f32 v228, 1.0, v228\n"
    "v_add_f32 v229, 1.0, v229\n"
    "v_mfma_f32_16x16x32_bf16 a[28:31], v[60:63], a[196:199], a[28:31]\n"
    "v_add_f32 v230, 1.0, v230\n"
    "v_add_f32 v231, 1.0, v231\n"
    "v_mfma_f32_16x16x32_bf16 a[28:31], v[44:47], a[192:195], a[28:31]\n"
    "v_rcp_f32 v228, v228\n"
    "v_mfma_f32_16x16x32_bf16 a[28:31], v[240:243], a[188:191], a[28:31]\n"
    "v_rcp_f32 v229, v229\n"
    "v_mfma_f32_16x16x32_bf16 a[8:11], v[212:215], a[196:199], a[8:11]\n"
    "v_rcp_f32 v230, v230\n"
    "v_mfma_f32_16x16x32_bf16 a[8:11], v[88:91], a[216:219], a[8:11]\n"
    "v_rcp_f32 v231, v231\n"
    "v_mfma_f32_16x16x32_bf16 a[8:11], a[240:243], a[220:223], a[8:11]\n"
    "v_fma_f32 v228, v228, s85, v34\n"
    "v_fma_f32 v229, v229, s85, v34\n"
    "v_mfma_f32_16x16x32_bf16 a[24:27], a[240:243], a[224:227], a[24:27]\n"
    "v_fma_f32 v230, v230, s85, v34\n"
    "v_fma_f32 v231, v231, s85, v34\n"
    "v_mfma_f32_16x16x32_bf16 a[24:27], v[88:91], a[220:223], a[24:27]\n"
    "v_cvt_pk_bf16_f32 v204, v228, v229\n"
    "v_cvt_pk_bf16_f32 v206, v230, v231\n"
    "v_mfma_f32_16x16x32_bf16 a[24:27], v[212:215], a[216:219], a[24:27]\n"
    "global_store_dwordx4 v24, v[204:207], s[78:79] offset:2048 sc1\n"
    "s_waitcnt lgkmcnt(0)\n"
    "v_add_f32 v244, v244, v188\n"
    "v_mfma_f32_16x16x32_bf16 a[12:15], v[76:79], a[196:199], a[12:15]\n"
    "v_add_f32 v245, v245, v189\n"
    "v_add_f32 v246, v246, v190\n"
    "v_mfma_f32_16x16x32_bf16 a[12:15], v[124:127], a[216:219], a[12:15]\n"
    "v_add_f32 v247, v247, v191\n"
    "v_add_f32 v52, v52, v168\n"
    "v_mfma_f32_16x16x32_bf16 a[12:15], a[244:247], a[220:223], a[12:15]\n"
    "v_add_f32 v53, v53, v169\n"
    "v_add_f32 v54, v54, v170\n"
    "v_mfma_f32_16x16x32_bf16 a[28:31], a[244:247], a[224:227], a[28:31]\n"
    "v_add_f32 v55, v55, v171\n"
    "v_mul_f32 v244, s84, v244\n"
    "v_mfma_f32_16x16x32_bf16 a[28:31], v[124:127], a[220:223], a[28:31]\n"
    "v_mul_f32 v245, s84, v245\n"
    "v_mul_f32 v246, s84, v246\n"
    "v_mfma_f32_16x16x32_bf16 a[28:31], v[76:79], a[216:219], a[28:31]\n"
    "v_mul_f32 v247, s84, v247\n"
    "v_fma_f32 v244, v52, s84, v244\n"
    "v_mfma_f32_16x16x32_bf16 a[8:11], a[248:251], a[224:227], a[8:11]\n"
    "v_fma_f32 v245, v53, s84, v245\n"
    "v_fma_f32 v246, v54, s84, v246\n"
    "v_mfma_f32_16x16x32_bf16 a[8:11], a[128:131], a[228:231], a[8:11]\n"
    "v_fma_f32 v247, v55, s84, v247\n"
    "v_mfma_f32_16x16x32_bf16 a[8:11], v[136:139], a[232:235], a[8:11]\n"
    "v_exp_f32 v244, v244\n"
    "v_mfma_f32_16x16x32_bf16 a[24:27], v[136:139], a[236:239], a[24:27]\n"
    "v_exp_f32 v245, v245\n"
    "v_mfma_f32_16x16x32_bf16 a[24:27], a[128:131], a[232:235], a[24:27]\n"
    "v_exp_f32 v246, v246\n"
    "v_mfma_f32_16x16x32_bf16 a[24:27], a[248:251], a[228:231], a[24:27]\n"
    "ds_write_b128 v28, a[8:11] offset:16384\n"
    "v_mfma_f32_16x16x32_bf16 a[12:15], a[132:135], a[224:227], a[12:15]\n"
    "v_exp_f32 v247, v247\n"
    "v_mfma_f32_16x16x32_bf16 a[12:15], a[252:255], a[228:231], a[12:15]\n"
    "v_add_f32 v244, 1.0, v244\n"
    "v_add_f32 v245, 1.0, v245\n"
    "v_mfma_f32_16x16x32_bf16 a[12:15], v[72:75], a[232:235], a[12:15]\n"
    "ds_write_b128 v28, a[24:27] offset:18432\n"
    "v_mfma_f32_16x16x32_bf16 a[28:31], v[72:75], a[236:239], a[28:31]\n"
    "v_add_f32 v246, 1.0, v246\n"
    "v_add_f32 v247, 1.0, v247\n"
    "v_mfma_f32_16x16x32_bf16 a[28:31], a[252:255], a[232:235], a[28:31]\n"
    "v_rcp_f32 v244, v244\n"
    "v_mfma_f32_16x16x32_bf16 a[28:31], a[132:135], a[228:231], a[28:31]\n"
    "ds_write_b128 v28, a[12:15] offset:17408\n"
    "L_st6_%=:\n"
    "s_cbranch_execz L_back_%=\n"
    "v_mfma_f32_16x16x32_bf16 a[40:43], a[140:143], a[136:139], a[40:43]\n"
    "v_rcp_f32 v245, v245\n"
    "v_mfma_f32_16x16x32_bf16 a[40:43], a[148:151], a[172:175], a[40:43]\n"
    "v_rcp_f32 v246, v246\n"
    "v_mfma_f32_16x16x32_bf16 a[40:43], a[168:171], a[176:179], a[40:43]\n"
    "ds_write_b128 v28, a[28:31] offset:19456\n"
    "global_load_dwordx4 v[192:195], v24, s[76:77] offset:0 sc1\n"
    "v_mfma_f32_16x16x32_bf16 a[56:59], a[168:171], a[180:183], a[56:59]\n"
    "v_rcp_f32 v247, v247\n"
    "v_mfma_f32_16x16x32_bf16 a[56:59], a[148:151], a[176:179], a[56:59]\n"
    "v_fma_f32 v244, v244, s85, v34\n"
    "v_fma_f32 v245, v245, s85, v34\n"
    "v_mfma_f32_16x16x32_bf16 a[56:59], a[140:143], a[172:175], a[56:59]\n"
    "v_fma_f32 v246, v246, s85, v34\n"
    "v_fma_f32 v247, v247, s85, v34\n"
    "v_mfma_f32_16x16x32_bf16 a[44:47], a[144:147], a[136:139], a[44:47]\n"
    "v_cvt_pk_bf16_f32 v236, v244, v245\n"
    "v_cvt_pk_bf16_f32 v238, v246, v247\n"
    "v_mfma_f32_16x16x32_bf16 a[44:47], a[156:159], a[172:175], a[44:47]\n"
    "global_store_dwordx4 v24, v[236:239], s[78:79] offset:3072 sc1\n"
    "v_mfma_f32_16x16x32_bf16 a[44:47], a[152:155], a[176:179], a[44:47]\n"
    "v_mfma_f32_16x16x32_bf16 a[60:63], a[152:155], a[180:183], a[60:63]\n"
    "s_waitcnt lgkmcnt(0)\n"
    "s_barrier\n"
    "v_mfma_f32_16x16x32_bf16 a[60:63], a[156:159], a[176:179], a[60:63]\n"
    "ds_read_b128 v[140:143], v29 offset:16384\n"
    "ds_read_b128 v[160:163], v30 offset:16384\n"
    "v_mfma_f32_16x16x32_bf16 a[60:63], a[144:147], a[172:175], a[60:63]\n"
    "ds_read_b128 v[176:179], v31 offset:16384\n"
    "ds_read_b128 v[108:111], v32 offset:16384\n"
    "v_mfma_f32_16x16x32_bf16 a[40:43], a[200:203], a[180:183], a[40:43]\n"
    "v_mfma_f32_16x16x32_bf16 a[40:43], a[208:211], a[184:187], a[40:43]\n"
    "v_mfma_f32_16x16x32_bf16 a[40:43], v[148:151], a[188:191], a[40:43]\n"
    "v_mfma_f32_16x16x32_bf16 a[56:59], v[148:151], a[192:195], a[56:59]\n"
    "v_mfma_f32_16x16x32_bf16 a[56:59], a[208:211], a[188:191], a[56:59]\n"
    "v_mfma_f32_16x16x32_bf16 a[56:59], a[200:203], a[184:187], a[56:59]\n"
    "v_mfma_f32_16x16x32_bf16 a[44:47], a[212:215], a[180:183], a[44:47]\n"
    "v_mfma_f32_16x16x32_bf16 a[44:47], a[204:207], a[184:187], a[44:47]\n"
    "s_waitcnt lgkmcnt(0)\n"
    "v_add_f32 v140, v140, v160\n"
    "v_add_f32 v141, v141, v161\n"
    "v_mfma_f32_16x16x32_bf16 a[44:47], v[152:155], a[188:191], a[44:47]\n"
    "v_add_f32 v142, v142, v162\n"
    "v_add_f32 v143, v143, v163\n"
    "v_mfma_f32_16x16x32_bf16 a[60:63], v[152:155], a[192:195], a[60:63]\n"
    "v_add_f32 v176, v176, v108\n"
    "v_add_f32 v177, v177, v109\n"
    "v_mfma_f32_16x16x32_bf16 a[60:63], a[204:207], a[188:191], a[60:63]\n"
    "v_add_f32 v178, v178, v110\n"
    "v_add_f32 v179, v179, v111\n"
    "v_mfma_f32_16x16x32_bf16 a[60:63], a[212:215], a[184:187], a[60:63]\n"
    "v_mul_f32 v140, s84, v140\n"
    "v_mul_f32 v141, s84, v141\n"
    "v_mfma_f32_16x16x32_bf16 a[40:43], v[164:167], a[192:195], a[40:43]\n"
    "v_mul_f32 v142, s84, v142\n"
    "v_mul_f32 v143, s84, v143\n"
    "v_mfma_f32_16x16x32_bf16 a[40:43], v[216:219], a[196:199], a[40:43]\n"
    "v_fma_f32 v140, v176, s84, v140\n"
    "v_fma_f32 v141, v177, s84, v141\n"
    "v_mfma_f32_16x16x32_bf16 a[40:43], v[196:199], a[216:219], a[40:43]\n"
    "v_fma_f32 v142, v178, s84, v142\n"
    "v_fma_f32 v143, v179, s84, v143\n"
    "v_mfma_f32_16x16x32_bf16 a[56:59], v[196:199], a[220:223], a[56:59]\n"
    "v_exp_f32 v140, v140\n"
    "v_mfma_f32_16x16x32_bf16 a[56:59], v[216:219], a[216:219], a[56:59]\n"
    "v_exp_f32 v141, v141\n"
    "v_mfma_f32_16x16x32_bf16 a[56:59], v[164:167], a[196:199], a[56:59]\n"
    "v_exp_f32 v142, v142\n"
    "v_mfma_f32_16x16x32_bf16 a[44:47], v[240:243], a[192:195], a[44:47]\n"
    "v_exp_f32 v143, v143\n"
    "global_load_dwordx4 v[80:83], v24, s[76:77] offset:1024 sc1\n"
    "v_mfma_f32_16x16x32_bf16 a[44:47], v[44:47], a[196:199], a[44:47]\n"
    "v_add_f32 v140, 1.0, v140\n"
    "v_add_f32 v141, 1.0, v141\n"
    "v_mfma_f32_16x16x32_bf16 a[44:47], v[60:63], a[216:219], a[44:47]\n"
    "v_add_f32 v142, 1.0, v142\n"
    "v_add_f32 v143, 1.0, v143\n"
    "v_mfma_f32_16x16x32_bf16 a[60:63], v[60:63], a[220:223], a[60:63]\n"
    "v_rcp_f32 v140, v140\n"
    "v_mfma_f32_16x16x32_bf16 a[60:63], v[44:47], a[216:219], a[60:63]\n"
    "v_rcp_f32 v141, v141\n"
    "v_mfma_f32_16x16x32_bf16 a[60:63], v[240:243], a[196:199], a[60:63]\n"
    "v_rcp_f32 v142, v142\n"
    "v_mfma_f32_16x16x32_bf16 a[40:43], v[212:215], a[220:223], a[40:43]\n"
    "v_rcp_f32 v143, v143\n"
    "v_mfma_f32_16x16x32_bf16 a[40:43], v[88:91], a[224:227], a[40:43]\n"
    "v_mfma_f32_16x16x32_bf16 a[40:43], a[240:243], a[228:231], a[40:43]\n"
    "v_mfma_f32_16x16x32_bf16 a[56:59], a[240:243], a[232:235], a[56:59]\n"
    "v_mfma_f32_16x16x32_bf16 a[56:59], v[88:91], a[228:231], a[56:59]\n"
    "v_mfma_f32_16x16x32_bf16 a[56:59], v[212:215], a[224:227], a[56:59]\n"
    "v_mfma_f32_16x16x32_bf16 a[44:47], v[76:79], a[220:223], a[44:47]\n"
    "v_mfma_f32_16x16x32_bf16 a[44:47], v[124:127], a[224:227], a[44:47]\n"
    "v_mfma_f32_16x16x32_bf16 a[44:47], a[244:247], a[228:231], a[44:47]\n"
    "v_mfma_f32_16x16x32_bf16 a[60:63], a[244:247], a[232:235], a[60:63]\n"
    "v_mfma_f32_16x16x32_bf16 a[60:63], v[124:127], a[228:231], a[60:63]\n"
    "v_mfma_f32_16x16x32_bf16 a[60:63], v[76:79], a[224:227], a[60:63]\n"
    "v_mfma_f32_16x16x32_bf16 a[40:43], a[248:251], a[232:235], a[40:43]\n"
    "v_mfma_f32_16x16x32_bf16 a[40:43], a[128:131], a[236:239], a[40:43]\n"
    "v_mfma_f32_16x16x32_bf16 a[40:43], v[136:139], v[104:107], a[40:43]\n"
    "v_mfma_f32_16x16x32_bf16 a[56:59], v[136:139], v[172:175], a[56:59]\n"
    "v_mfma_f32_16x16x32_bf16 a[56:59], a[128:131], v[104:107], a[56:59]\n"
    "v_mfma_f32_16x16x32_bf16 a[56:59], a[248:251], a[236:239], a[56:59]\n"
    "ds_write_b128 v28, a[40:43] offset:20480\n"
    "v_mfma_f32_16x16x32_bf16 a[44:47], a[132:135], a[232:235], a[44:47]\n"
    "v_mfma_f32_16x16x32_bf16 a[44:47], a[252:255], a[236:239], a[44:47]\n"
    "v_mfma_f32_16x16x32_bf16 a[44:47], v[72:75], v[104:107], a[44:47]\n"
    "ds_write_b128 v28, a[56:59] offset:22528\n"
    "v_mfma_f32_16x16x32_bf16 a[60:63], v[72:75], v[172:175], a[60:63]\n"
    "v_mfma_f32_16x16x32_bf16 a[60:63], a[252:255], v[104:107], a[60:63]\n"
    "v_mfma_f32_16x16x32_bf16 a[60:63], a[132:135], a[236:239], a[60:63]\n"
    "ds_write_b128 v28, a[44:47] offset:21504\n"
    "v_mfma_f32_16x16x32_bf16 a[72:75], a[140:143], a[176:179], a[72:75]\n"
    "v_mfma_f32_16x16x32_bf16 a[72:75], a[148:151], a[180:183], a[72:75]\n"
    "v_mfma_f32_16x16x32_bf16 a[72:75], a[168:171], a[184:187], a[72:75]\n"
    "ds_write_b128 v28, a[60:63] offset:23552\n"
    "v_mfma_f32_16x16x32_bf16 a[88:91], a[168:171], a[188:191], a[88:91]\n"
    "s_waitcnt vmcnt(2)\n"
    "v_xor_b32 v36, v193, v35\n"
    "v_bitop3_b32 v36, v195, v35, v36 bitop3:0xbe\n"
    "v_mfma_f32_16x16x32_bf16 a[88:91], a[148:151], a[184:187], a[88:91]\n"
    "v_lshlrev_b32 v64, 16, v192\n"
    "v_and_b32 v65, 0xffff0000, v192\n"
    "v_mfma_f32_16x16x32_bf16 a[88:91], a[140:143], a[180:183], a[88:91]\n"
    "v_lshlrev_b32 v66, 16, v194\n"
    "v_and_b32 v67, 0xffff0000, v194\n"
    "v_mfma_f32_16x16x32_bf16 a[76:79], a[144:147], a[176:179], a[76:79]\n"
    "v_fma_f32 v92, v140, s85, v34\n"
    "v_fma_f32 v93, v141, s85, v34\n"
    "v_mfma_f32_16x16x32_bf16 a[76:79], a[156:159], a[180:183], a[76:79]\n"
    "v_fma_f32 v94, v142, s85, v34\n"
    "v_fma_f32 v95, v143, s85, v34\n"
    "global_load_dwordx4 v[56:59], v24, s[76:77] offset:2048 sc1\n"
    "v_mfma_f32_16x16x32_bf16 a[76:79], a[152:155], a[184:187], a[76:79]\n"
    "v_mul_f32 v92, v92, v64\n"
    "v_mul_f32 v93, v93, v65\n"
    "v_mfma_f32_16x16x32_bf16 a[92:95], a[152:155], a[188:191], a[92:95]\n"
    "s_waitcnt lgkmcnt(0)\n"
    "s_barrier\n"
    "v_mfma_f32_16x16x32_bf16 a[92:95], a[156:159], a[184:187], a[92:95]\n"
    "ds_read_b128 v[68:71], v29 offset:20480\n"
    "ds_read_b128 v[248:251], v30 offset:20480\n"
    "v_mfma_f32_16x16x32_bf16 a[92:95], a[144:147], a[180:183], a[92:95]\n"
    "ds_read_b128 v[156:159], v31 offset:20480\n"
    "ds_read_b128 v[96:99], v32 offset:20480\n"
    "v_mfma_f32_16x16x32_bf16 a[72:75], a[200:203], a[188:191], a[72:75]\n"
    "v_mul_f32 v94, v94, v66\n"
    "v_mul_f32 v95, v95, v67\n"
    "v_mfma_f32_16x16x32_bf16 a[72:75], a[208:211], a[192:195], a[72:75]\n"
    "v_cvt_pk_bf16_f32 v92, v92, v93\n"
    "v_cvt_pk_bf16_f32 v93, v94, v95\n"
    "v_mfma_f32_16x16x32_bf16 a[72:75], v[148:151], a[196:199], a[72:75]\n"
    "buffer_store_dwordx2 v[92:93], v33, s[48:51], 0 offen\n"
    "v_mfma_f32_16x16x32_bf16 a[88:91], v[148:151], a[216:219], a[88:91]\n"
    "v_mfma_f32_16x16x32_bf16 a[88:91], a[208:211], a[196:199], a[88:91]\n"
    "v_mfma_f32_16x16x32_bf16 a[88:91], a[200:203], a[192:195], a[88:91]\n"
    "v_mfma_f32_16x16x32_bf16 a[76:79], a[212:215], a[188:191], a[76:79]\n"
    "v_mfma_f32_16x16x32_bf16 a[76:79], a[204:207], a[192:195], a[76:79]\n"
    "s_waitcnt lgkmcnt(0)\n"
    "v_add_f32 v68, v68, v248\n"
    "v_add_f32 v69, v69, v249\n"
    "v_mfma_f32_16x16x32_bf16 a[76:79], v[152:155], a[196:199], a[76:79]\n"
    "v_add_f32 v70, v70, v250\n"
    "v_add_f32 v71, v71, v251\n"
    "v_mfma_f32_16x16x32_bf16 a[92:95], v[152:155], a[216:219], a[92:95]\n"
    "v_add_f32 v156, v156, v96\n"
    "v_add_f32 v157, v157, v97\n"
    "v_mfma_f32_16x16x32_bf16 a[92:95], a[204:207], a[196:199], a[92:95]\n"
    "v_add_f32 v158, v158, v98\n"
    "v_add_f32 v159, v159, v99\n"
    "v_mfma_f32_16x16x32_bf16 a[92:95], a[212:215], a[192:195], a[92:95]\n"
    "v_mul_f32 v68, s84, v68\n"
    "v_mul_f32 v69, s84, v69\n"
    "v_mfma_f32_16x16x32_bf16 a[72:75], v[164:167], a[216:219], a[72:75]\n"
    "v_mul_f32 v70, s84, v70\n"
    "v_mul_f32 v71, s84, v71\n"
    "v_mfma_f32_16x16x32_bf16 a[72:75], v[216:219], a[220:223], a[72:75]\n"
    "v_fma_f32 v68, v156, s84, v68\n"
    "v_fma_f32 v69, v157, s84, v69\n"
    "v_mfma_f32_16x16x32_bf16 a[72:75], v[196:199], a[224:227], a[72:75]\n"
    "v_fma_f32 v70, v158, s84, v70\n"
    "v_fma_f32 v71, v159, s84, v71\n"
    "v_mfma_f32_16x16x32_bf16 a[88:91], v[196:199], a[228:231], a[88:91]\n"
    "v_exp_f32 v68, v68\n"
    "v_mfma_f32_16x16x32_bf16 a[88:91], v[216:219], a[224:227], a[88:91]\n"
    "v_exp_f32 v69, v69\n"
    "v_mfma_f32_16x16x32_bf16 a[88:91], v[164:167], a[220:223], a[88:91]\n"
    "v_exp_f32 v70, v70\n"
    "v_mfma_f32_16x16x32_bf16 a[76:79], v[240:243], a[216:219], a[76:79]\n"
    "v_exp_f32 v71, v71\n"
    "v_mfma_f32_16x16x32_bf16 a[76:79], v[44:47], a[220:223], a[76:79]\n"
    "v_add_f32 v68, 1.0, v68\n"
    "v_add_f32 v69, 1.0, v69\n"
    "v_mfma_f32_16x16x32_bf16 a[76:79], v[60:63], a[224:227], a[76:79]\n"
    "v_add_f32 v70, 1.0, v70\n"
    "v_add_f32 v71, 1.0, v71\n"
    "v_mfma_f32_16x16x32_bf16 a[92:95], v[60:63], a[228:231], a[92:95]\n"
    "v_rcp_f32 v68, v68\n"
    "v_mfma_f32_16x16x32_bf16 a[92:95], v[44:47], a[224:227], a[92:95]\n"
    "v_rcp_f32 v69, v69\n"
    "v_mfma_f32_16x16x32_bf16 a[92:95], v[240:243], a[220:223], a[92:95]\n"
    "v_rcp_f32 v70, v70\n"
    "v_mfma_f32_16x16x32_bf16 a[72:75], v[212:215], a[228:231], a[72:75]\n"
    "v_rcp_f32 v71, v71\n"
    "v_mfma_f32_16x16x32_bf16 a[72:75], v[88:91], a[232:235], a[72:75]\n"
    "s_waitcnt vmcnt(2)\n"
    "v_bitop3_b32 v36, v81, v35, v36 bitop3:0xbe\n"
    "v_bitop3_b32 v36, v83, v35, v36 bitop3:0xbe\n"
    "v_mfma_f32_16x16x32_bf16 a[72:75], a[240:243], a[236:239], a[72:75]\n"
    "v_lshlrev_b32 v128, 16, v80\n"
    "v_and_b32 v129, 0xffff0000, v80\n"
    "v_mfma_f32_16x16x32_bf16 a[88:91], a[240:243], v[104:107], a[88:91]\n"
    "v_lshlrev_b32 v130, 16, v82\n"
    "v_and_b32 v131, 0xffff0000, v82\n"
    "v_mfma_f32_16x16x32_bf16 a[88:91], v[88:91], a[236:239], a[88:91]\n"
    "v_fma_f32 v184, v68, s85, v34\n"
    "v_fma_f32 v185, v69, s85, v34\n"
    "v_mfma_f32_16x16x32_bf16 a[88:91], v[212:215], a[232:235], a[88:91]\n"
    "v_fma_f32 v186, v70, s85, v34\n"
    "v_fma_f32 v187, v71, s85, v34\n"
    "v_mfma_f32_16x16x32_bf16 a[76:79], v[76:79], a[228:231], a[76:79]\n"
    "v_mul_f32 v184, v184, v128\n"
    "v_mul_f32 v185, v185, v129\n"
    "global_load_dwordx4 v[100:103], v24, s[76:77] offset:3072 sc1\n"
    "v_mfma_f32_16x16x32_bf16 a[76:79], v[124:127], a[232:235], a[76:79]\n"
    "v_mul_f32 v186, v186, v130\n"
    "v_mul_f32 v187, v187, v131\n"
    "v_mfma_f32_16x16x32_bf16 a[76:79], a[244:247], a[236:239], a[76:79]\n"
    "v_cvt_pk_bf16_f32 v184, v184, v185\n"
    "v_cvt_pk_bf16_f32 v185, v186, v187\n"
    "v_mfma_f32_16x16x32_bf16 a[92:95], a[244:247], v[104:107], a[92:95]\n"
    "buffer_store_dwordx2 v[184:185], v37, s[48:51], 0 offen\n"
    "v_mfma_f32_16x16x32_bf16 a[92:95], v[124:127], a[236:239], a[92:95]\n"
    "v_mfma_f32_16x16x32_bf16 a[92:95], v[76:79], a[232:235], a[92:95]\n"
    "v_mfma_f32_16x16x32_bf16 a[72:75], a[248:251], v[104:107], a[72:75]\n"
    "v_mfma_f32_16x16x32_bf16 a[72:75], a[128:131], v[172:175], a[72:75]\n"
    "v_mfma_f32_16x16x32_bf16 a[72:75], v[136:139], v[144:147], a[72:75]\n"
    "v_mfma_f32_16x16x32_bf16 a[88:91], v[136:139], v[180:183], a[88:91]\n"
    "v_mfma_f32_16x16x32_bf16 a[88:91], a[128:131], v[144:147], a[88:91]\n"
    "v_mfma_f32_16x16x32_bf16 a[88:91], a[248:251], v[172:175], a[88:91]\n"
    "ds_write_b128 v28, a[72:75] offset:24576\n"
    "v_mfma_f32_16x16x32_bf16 a[76:79], a[132:135], v[104:107], a[76:79]\n"
    "v_mfma_f32_16x16x32_bf16 a[76:79], a[252:255], v[172:175], a[76:79]\n"
    "v_mfma_f32_16x16x32_bf16 a[76:79], v[72:75], v[144:147], a[76:79]\n"
    "ds_write_b128 v28, a[88:91] offset:26624\n"
    "v_mfma_f32_16x16x32_bf16 a[92:95], v[72:75], v[180:183], a[92:95]\n"
    "v_mfma_f32_16x16x32_bf16 a[92:95], a[252:255], v[144:147], a[92:95]\n"
    "v_mfma_f32_16x16x32_bf16 a[92:95], a[132:135], v[172:175], a[92:95]\n"
    "ds_write_b128 v28, a[76:79] offset:25600\n"
    "v_mfma_f32_16x16x32_bf16 a[104:107], a[140:143], a[184:187], a[104:107]\n"
    "v_mfma_f32_16x16x32_bf16 a[104:107], a[148:151], a[188:191], a[104:107]\n"
    "v_mfma_f32_16x16x32_bf16 a[104:107], a[168:171], a[192:195], a[104:107]\n"
    "ds_write_b128 v28, a[92:95] offset:27648\n"
    "v_mfma_f32_16x16x32_bf16 a[120:123], a[168:171], a[196:199], a[120:123]\n"
    "v_mfma_f32_16x16x32_bf16 a[120:123], a[148:151], a[192:195], a[120:123]\n"
    "v_mfma_f32_16x16x32_bf16 a[120:123], a[140:143], a[188:191], a[120:123]\n"
    "v_mfma_f32_16x16x32_bf16 a[108:111], a[144:147], a[184:187], a[108:111]\n"
    "v_mfma_f32_16x16x32_bf16 a[108:111], a[156:159], a[188:191], a[108:111]\n"
    "s_waitcnt vmcnt(3)\n"
    "v_bitop3_b32 v36, v57, v35, v36 bitop3:0xbe\n"
    "v_bitop3_b32 v36, v59, v35, v36 bitop3:0xbe\n"
    "v_mfma_f32_16x16x32_bf16 a[108:111], a[152:155], a[192:195], a[108:111]\n"
    "v_lshlrev_b32 v116, 16, v56\n"
    "v_and_b32 v117, 0xffff0000, v56\n"
    "v_mfma_f32_16x16x32_bf16 a[124:127], a[152:155], a[196:199], a[124:127]\n"
    "s_waitcnt lgkmcnt(0)\n"
    "s_barrier\n"
    "v_mfma_f32_16x16x32_bf16 a[124:127], a[156:159], a[192:195], a[124:127]\n"
    "ds_read_b128 v[132:135], v29 offset:24576\n"
    "ds_read_b128 v[112:115], v30 offset:24576\n"
    "v_mfma_f32_16x16x32_bf16 a[124:127], a[144:147], a[188:191], a[124:127]\n"
    "ds_read_b128 v[208:211], v31 offset:24576\n"
    "ds_read_b128 v[84:87], v32 offset:24576\n"
    "v_mfma_f32_16x16x32_bf16 a[104:107], a[200:203], a[196:199], a[104:107]\n"
    "v_lshlrev_b32 v118, 16, v58\n"
    "v_and_b32 v119, 0xffff0000, v58\n"
    "v_mfma_f32_16x16x32_bf16 a[104:107], a[208:211], a[216:219], a[104:107]\n"
    "v_mfma_f32_16x16x32_bf16 a[104:107], v[148:151], a[220:223], a[104:107]\n"
    "v_mfma_f32_16x16x32_bf16 a[120:123], v[148:151], a[224:227], a[120:123]\n"
    "v_mfma_f32_16x16x32_bf16 a[120:123], a[208:211], a[220:223], a[120:123]\n"
    "v_mfma_f32_16x16x32_bf16 a[120:123], a[200:203], a[216:219], a[120:123]\n"
    "v_mfma_f32_16x16x32_bf16 a[108:111], a[212:215], a[196:199], a[108:111]\n"
    "v_mfma_f32_16x16x32_bf16 a[108:111], a[204:207], a[216:219], a[108:111]\n"
    "s_waitcnt lgkmcnt(0)\n"
    "v_add_f32 v132, v132, v112\n"
    "v_add_f32 v133, v133, v113\n"
    "v_mfma_f32_16x16x32_bf16 a[108:111], v[152:155], a[220:223], a[108:111]\n"
    "v_add_f32 v134, v134, v114\n"
    "v_add_f32 v135, v135, v115\n"
    "v_mfma_f32_16x16x32_bf16 a[124:127], v[152:155], a[224:227], a[124:127]\n"
    "v_add_f32 v208, v208, v84\n"
    "v_add_f32 v209, v209, v85\n"
    "v_mfma_f32_16x16x32_bf16 a[124:127], a[204:207], a[220:223], a[124:127]\n"
    "v_add_f32 v210, v210, v86\n"
    "v_add_f32 v211, v211, v87\n"
    "v_mfma_f32_16x16x32_bf16 a[124:127], a[212:215], a[216:219], a[124:127]\n"
    "v_mul_f32 v132, s84, v132\n"
    "v_mul_f32 v133, s84, v133\n"
    "v_mfma_f32_16x16x32_bf16 a[104:107], v[164:167], a[224:227], a[104:107]\n"
    "v_mul_f32 v134, s84, v134\n"
    "v_mul_f32 v135, s84, v135\n"
    "v_mfma_f32_16x16x32_bf16 a[104:107], v[216:219], a[228:231], a[104:107]\n"
    "v_fma_f32 v132, v208, s84, v132\n"
    "v_fma_f32 v133, v209, s84, v133\n"
    "v_mfma_f32_16x16x32_bf16 a[104:107], v[196:199], a[232:235], a[104:107]\n"
    "v_fma_f32 v134, v210, s84, v134\n"
    "v_fma_f32 v135, v211, s84, v135\n"
    "v_mfma_f32_16x16x32_bf16 a[120:123], v[196:199], a[236:239], a[120:123]\n"
    "v_exp_f32 v132, v132\n"
    "v_mfma_f32_16x16x32_bf16 a[120:123], v[216:219], a[232:235], a[120:123]\n"
    "v_exp_f32 v133, v133\n"
    "v_mfma_f32_16x16x32_bf16 a[120:123], v[164:167], a[228:231], a[120:123]\n"
    "v_exp_f32 v134, v134\n"
    "v_mfma_f32_16x16x32_bf16 a[108:111], v[240:243], a[224:227], a[108:111]\n"
    "v_exp_f32 v135, v135\n"
    "v_mfma_f32_16x16x32_bf16 a[108:111], v[44:47], a[228:231], a[108:111]\n"
    "v_add_f32 v132, 1.0, v132\n"
    "v_add_f32 v133, 1.0, v133\n"
    "v_mfma_f32_16x16x32_bf16 a[108:111], v[60:63], a[232:235], a[108:111]\n"
    "v_add_f32 v134, 1.0, v134\n"
    "v_add_f32 v135, 1.0, v135\n"
    "v_mfma_f32_16x16x32_bf16 a[124:127], v[60:63], a[236:239], a[124:127]\n"
    "v_rcp_f32 v132, v132\n"
    "v_mfma_f32_16x16x32_bf16 a[124:127], v[44:47], a[232:235], a[124:127]\n"
    "v_rcp_f32 v133, v133\n"
    "v_mfma_f32_16x16x32_bf16 a[124:127], v[240:243], a[228:231], a[124:127]\n"
    "v_rcp_f32 v134, v134\n"
    "v_mfma_f32_16x16x32_bf16 a[104:107], v[212:215], a[236:239], a[104:107]\n"
    "v_rcp_f32 v135, v135\n"
    "v_mfma_f32_16x16x32_bf16 a[104:107], v[88:91], v[104:107], a[104:107]\n"
    "v_fma_f32 v252, v132, s85, v34\n"
    "v_fma_f32 v253, v133, s85, v34\n"
    "v_mfma_f32_16x16x32_bf16 a[104:107], a[240:243], v[172:175], a[104:107]\n"
    "v_fma_f32 v254, v134, s85, v34\n"
    "v_fma_f32 v255, v135, s85, v34\n"
    "v_mfma_f32_16x16x32_bf16 a[120:123], a[240:243], v[144:147], a[120:123]\n"
    "v_mul_f32 v252, v252, v116\n"
    "v_mul_f32 v253, v253, v117\n"
    "v_mfma_f32_16x16x32_bf16 a[120:123], v[88:91], v[172:175], a[120:123]\n"
    "v_mul_f32 v254, v254, v118\n"
    "v_mul_f32 v255, v255, v119\n"
    "v_mfma_f32_16x16x32_bf16 a[120:123], v[212:215], v[104:107], a[120:123]\n"
    "v_cvt_pk_bf16_f32 v252, v252, v253\n"
    "v_cvt_pk_bf16_f32 v253, v254, v255\n"
    "v_mfma_f32_16x16x32_bf16 a[108:111], v[76:79], a[236:239], a[108:111]\n"
    "buffer_store_dwordx2 v[252:253], v38, s[48:51], 0 offen\n"
    "s_waitcnt vmcnt(2)\n"
    "v_bitop3_b32 v36, v101, v35, v36 bitop3:0xbe\n"
    "v_mfma_f32_16x16x32_bf16 a[108:111], v[124:127], v[104:107], a[108:111]\n"
    "v_bitop3_b32 v36, v103, v35, v36 bitop3:0xbe\n"
    "v_lshlrev_b32 v48, 16, v100\n"
    "v_mfma_f32_16x16x32_bf16 a[108:111], a[244:247], v[172:175], a[108:111]\n"
    "v_and_b32 v49, 0xffff0000, v100\n"
    "v_lshlrev_b32 v50, 16, v102\n"
    "v_mfma_f32_16x16x32_bf16 a[124:127], a[244:247], v[144:147], a[124:127]\n"
    "v_and_b32 v51, 0xffff0000, v102\n"
    "v_mul_f32 v232, s85, v48\n"
    "v_mfma_f32_16x16x32_bf16 a[124:127], v[124:127], v[172:175], a[124:127]\n"
    "v_mul_f32 v233, s85, v49\n"
    "v_mul_f32 v234, s85, v50\n"
    "v_mfma_f32_16x16x32_bf16 a[124:127], v[76:79], v[104:107], a[124:127]\n"
    "v_mul_f32 v235, s85, v51\n"
    "v_mul_f32 v48, s86, v48\n"
    "v_mfma_f32_16x16x32_bf16 a[104:107], a[248:251], v[144:147], a[104:107]\n"
    "v_mul_f32 v49, s86, v49\n"
    "v_mul_f32 v50, s86, v50\n"
    "v_mfma_f32_16x16x32_bf16 a[104:107], a[128:131], v[180:183], a[104:107]\n"
    "v_mul_f32 v51, s86, v51\n"
    "v_cmp_ne_u32 vcc, 0, v36\n"
    "v_mfma_f32_16x16x32_bf16 a[104:107], v[136:139], v[200:203], a[104:107]\n"
    "v_mfma_f32_16x16x32_bf16 a[120:123], v[136:139], v[224:227], a[120:123]\n"
    "v_mfma_f32_16x16x32_bf16 a[120:123], a[128:131], v[200:203], a[120:123]\n"
    "v_mfma_f32_16x16x32_bf16 a[120:123], a[248:251], v[180:183], a[120:123]\n"
    "ds_write_b128 v28, a[104:107] offset:28672\n"
    "v_mfma_f32_16x16x32_bf16 a[108:111], a[132:135], v[144:147], a[108:111]\n"
    "v_mfma_f32_16x16x32_bf16 a[108:111], a[252:255], v[180:183], a[108:111]\n"
    "v_mfma_f32_16x16x32_bf16 a[108:111], v[72:75], v[200:203], a[108:111]\n"
    "ds_write_b128 v28, a[120:123] offset:30720\n"
    "v_mfma_f32_16x16x32_bf16 a[124:127], v[72:75], v[224:227], a[124:127]\n"
    "v_mfma_f32_16x16x32_bf16 a[124:127], a[252:255], v[200:203], a[124:127]\n"
    "v_mfma_f32_16x16x32_bf16 a[124:127], a[132:135], v[180:183], a[124:127]\n"
    "ds_write_b128 v28, a[108:111] offset:29696\n"
    "s_nop 7\n"
    "s_nop 2\n"
    "ds_write_b128 v28, a[124:127] offset:31744\n"
    "s_waitcnt lgkmcnt(0)\n"
    "s_barrier\n"
    "ds_read_b128 v[228:231], v29 offset:28672\n"
    "ds_read_b128 v[188:191], v30 offset:28672\n"
    "ds_read_b128 v[168:171], v31 offset:28672\n"
    "ds_read_b128 v[52:55], v32 offset:28672\n"
    "s_waitcnt lgkmcnt(2)\n"
    "v_add_f32 v228, v228, v188\n"
    "v_add_f32 v229, v229, v189\n"
    "v_add_f32 v230, v230, v190\n"
    "v_add_f32 v231, v231, v191\n"
    "s_waitcnt lgkmcnt(0)\n"
    "v_add_f32 v168, v168, v52\n"
    "v_add_f32 v169, v169, v53\n"
    "v_add_f32 v170, v170, v54\n"
    "v_add_f32 v171, v171, v55\n"
    "v_mul_f32 v228, s84, v228\n"
    "v_mul_f32 v229, s84, v229\n"
    "v_mul_f32 v230, s84, v230\n"
    "v_mul_f32 v231, s84, v231\n"
    "v_fma_f32 v228, v168, s84, v228\n"
    "v_fma_f32 v229, v169, s84, v229\n"
    "v_fma_f32 v230, v170, s84, v230\n"
    "v_fma_f32 v231, v171, s84, v231\n"
    "v_exp_f32 v228, v228\n"
    "v_exp_f32 v229, v229\n"
    "v_exp_f32 v230, v230\n"
    "v_exp_f32 v231, v231\n"
    "v_add_f32 v228, 1.0, v228\n"
    "v_add_f32 v229, 1.0, v229\n"
    "v_add_f32 v230, 1.0, v230\n"
    "v_add_f32 v231, 1.0, v231\n"
    "v_rcp_f32 v228, v228\n"
    "v_rcp_f32 v229, v229\n"
    "v_rcp_f32 v230, v230\n"
    "v_rcp_f32 v231, v231\n"
    "v_fma_f32 v232, v228, v232, v48\n"
    "v_fma_f32 v233, v229, v233, v49\n"
    "v_fma_f32 v234, v230, v234, v50\n"
    "v_fma_f32 v235, v231, v235, v51\n"
    "v_cvt_pk_bf16_f32 v232, v232, v233\n"
    "v_cvt_pk_bf16_f32 v233, v234, v235\n"
    "buffer_store_dwordx2 v[232:233], v39, s[48:51], 0 offen\n"
    "s_cbranch_vccnz L_slow_%=\n"
    "s_endpgm\n"
    "L_slow_%=:\n"
    "s_mov_b32 s87, 0x40000\n"
    "L_poll_%=:\n"
    "global_load_dwordx4 v[44:47], v24, s[76:77] offset:0 sc1\n"
    "global_load_dwordx4 v[48:51], v24, s[76:77] offset:1024 sc1\n"
    "global_load_dwordx4 v[52:55], v24, s[76:77] offset:2048 sc1\n"
    "global_load_dwordx4 v[56:59], v24, s[76:77] offset:3072 sc1\n"
    "s_waitcnt vmcnt(0)\n"
    "v_xor_b32 v36, v45, v26\n"
    "v_bitop3_b32 v36, v47, v26, v36 bitop3:0xbe\n"
    "v_bitop3_b32 v36, v49, v26, v36 bitop3:0xbe\n"
    "v_bitop3_b32 v36, v51, v26, v36 bitop3:0xbe\n"
    "v_bitop3_b32 v36, v53, v26, v36 bitop3:0xbe\n"
    "v_bitop3_b32 v36, v55, v26, v36 bitop3:0xbe\n"
    "v_bitop3_b32 v36, v57, v26, v36 bitop3:0xbe\n"
    "v_bitop3_b32 v36, v59, v26, v36 bitop3:0xbe\n"
    "v_and_b32 v36, 31, v36\n"
    "v_cmp_ne_u32 vcc, 0, v36\n"
    "s_cbranch_vccz L_got_%=\n"
    "s_sub_u32 s87, s87, 1\n"
    "s_cmp_eq_u32 s87, 0\n"
    "s_cbranch_scc1 L_got_%=\n"
    "global_load_dword v36, v25, s[82:83] sc1\n"
    "s_waitcnt vmcnt(0)\n"
    "v_and_b32 v36, 31, v36\n"
    "v_cmp_ne_u32 vcc, v36, v27\n"
    "s_cbranch_vccz L_poll_%=\n"
    "v_mov_b32 v27, v36\n"
    "v_lshl_or_b32 v221, v26, 8, v27\n"
    "v_lshl_or_b32 v223, v26, 8, v27\n"
    "v_lshl_or_b32 v121, v26, 8, v27\n"
    "v_lshl_or_b32 v123, v26, 8, v27\n"
    "v_lshl_or_b32 v205, v26, 8, v27\n"
    "v_lshl_or_b32 v207, v26, 8, v27\n"
    "v_lshl_or_b32 v237, v26, 8, v27\n"
    "v_lshl_or_b32 v239, v26, 8, v27\n"
    "global_store_dwordx4 v24, v[220:223], s[78:79] offset:0 sc1\n"
    "global_store_dwordx4 v24, v[120:123], s[78:79] offset:1024 sc1\n"
    "global_store_dwordx4 v24, v[204:207], s[78:79] offset:2048 sc1\n"
    "global_store_dwordx4 v24, v[236:239], s[78:79] offset:3072 sc1\n"
    "s_branch L_poll_%=\n"
    "L_got_%=:\n"
    "v_bfe_u32 v36, v45, 8, 5\n"
    "v_cmp_ne_u32 vcc, v36, v27\n"
    "s_cbranch_vccz L_got2_%=\n"
    "v_mov_b32 v27, v36\n"
    "v_lshl_or_b32 v221, v26, 8, v27\n"
    "v_lshl_or_b32 v223, v26, 8, v27\n"
    "v_lshl_or_b32 v121, v26, 8, v27\n"
    "v_lshl_or_b32 v123, v26, 8, v27\n"
    "v_lshl_or_b32 v205, v26, 8, v27\n"
    "v_lshl_or_b32 v207, v26, 8, v27\n"
    "v_lshl_or_b32 v237, v26, 8, v27\n"
    "v_lshl_or_b32 v239, v26, 8, v27\n"
    "global_store_dwordx4 v24, v[220:223], s[78:79] offset:0 sc1\n"
    "global_store_dwordx4 v24, v[120:123], s[78:79] offset:1024 sc1\n"
    "global_store_dwordx4 v24, v[204:207], s[78:79] offset:2048 sc1\n"
    "global_store_dwordx4 v24, v[236:239], s[78:79] offset:3072 sc1\n"
    "L_got2_%=:\n"
    "v_lshlrev_b32 v60, 16, v44\n"
    "v_and_b32 v61, 0xffff0000, v44\n"
    "v_lshlrev_b32 v62, 16, v46\n"
    "v_and_b32 v63, 0xffff0000, v46\n"
    "v_mul_f32 v44, s86, v60\n"
    "v_mul_f32 v45, s86, v61\n"
    "v_mul_f32 v46, s86, v62\n"
    "v_mul_f32 v47, s86, v63\n"
    "v_mul_f32 v60, s85, v60\n"
    "v_mul_f32 v61, s85, v61\n"
    "v_mul_f32 v62, s85, v62\n"
    "v_mul_f32 v63, s85, v63\n"
    "v_fma_f32 v60, v140, v60, v44\n"
    "v_fma_f32 v61, v141, v61, v45\n"
    "v_fma_f32 v62, v142, v62, v46\n"
    "v_fma_f32 v63, v143, v63, v47\n"
    "v_cvt_pk_bf16_f32 v60, v60, v61\n"
    "v_cvt_pk_bf16_f32 v61, v62, v63\n"
    "buffer_store_dwordx2 v[60:61], v33, s[48:51], 0 offen\n"
    "v_lshlrev_b32 v64, 16, v48\n"
    "v_and_b32 v65, 0xffff0000, v48\n"
    "v_lshlrev_b32 v66, 16, v50\n"
    "v_and_b32 v67, 0xffff0000, v50\n"
    "v_mul_f32 v48, s86, v64\n"
    "v_mul_f32 v49, s86, v65\n"
    "v_mul_f32 v50, s86, v66\n"
    "v_mul_f32 v51, s86, v67\n"
    "v_mul_f32 v64, s85, v64\n"
    "v_mul_f32 v65, s85, v65\n"
    "v_mul_f32 v66, s85, v66\n"
    "v_mul_f32 v67, s85, v67\n"
    "v_fma_f32 v64, v68, v64, v48\n"
    "v_fma_f32 v65, v69, v65, v49\n"
    "v_fma_f32 v66, v70, v66, v50\n"
    "v_fma_f32 v67, v71, v67, v51\n"
    "v_cvt_pk_bf16_f32 v64, v64, v65\n"
    "v_cvt_pk_bf16_f32 v65, v66, v67\n"
    "buffer_store_dwordx2 v[64:65], v37, s[48:51], 0 offen\n"
    "v_lshlrev_b32 v72, 16, v52\n"
    "v_and_b32 v73, 0xffff0000, v52\n"
    "v_lshlrev_b32 v74, 16, v54\n"
    "v_and_b32 v75, 0xffff0000, v54\n"
    "v_mul_f32 v52, s86, v72\n"
    "v_mul_f32 v53, s86, v73\n"
    "v_mul_f32 v54, s86, v74\n"
    "v_mul_f32 v55, s86, v75\n"
    "v_mul_f32 v72, s85, v72\n"
    "v_mul_f32 v73, s85, v73\n"
    "v_mul_f32 v74, s85, v74\n"
    "v_mul_f32 v75, s85, v75\n"
    "v_fma_f32 v72, v132, v72, v52\n"
    "v_fma_f32 v73, v133, v73, v53\n"
    "v_fma_f32 v74, v134, v74, v54\n"
    "v_fma_f32 v75, v135, v75, v55\n"
    "v_cvt_pk_bf16_f32 v72, v72, v73\n"
    "v_cvt_pk_bf16_f32 v73, v74, v75\n"
    "buffer_store_dwordx2 v[72:73], v38, s[48:51], 0 offen\n"
    "v_lshlrev_b32 v76, 16, v56\n"
    "v_and_b32 v77, 0xffff0000, v56\n"
    "v_lshlrev_b32 v78, 16, v58\n"
    "v_and_b32 v79, 0xffff0000, v58\n"
    "v_mul_f32 v56, s86, v76\n"
    "v_mul_f32 v57, s86, v77\n"
    "v_mul_f32 v58, s86, v78\n"
    "v_mul_f32 v59, s86, v79\n"
    "v_mul_f32 v76, s85, v76\n"
    "v_mul_f32 v77, s85, v77\n"
    "v_mul_f32 v78, s85, v78\n"
    "v_mul_f32 v79, s85, v79\n"
    "v_fma_f32 v76, v228, v76, v56\n"
    "v_fma_f32 v77, v229, v77, v57\n"
    "v_fma_f32 v78, v230, v78, v58\n"
    "v_fma_f32 v79, v231, v79, v59\n"
    "v_cvt_pk_bf16_f32 v76, v76, v77\n"
    "v_cvt_pk_bf16_f32 v77, v78, v79\n"
    "buffer_store_dwordx2 v[76:77], v39, s[48:51], 0 offen\n"
    "L_fin_%=:\n"
    : "+v"(tid)
    : [ka]"s"(ka), [bid]"s"(bid), [lds]"s"(lds0)
    : "a0","a1","a2","a3","a4","a5","a6","a7","a8","a9","a10","a11","a12","a13","a14","a15","a16","a17","a18","a19","a20","a21","a22","a23","a24","a25","a26","a27","a28","a29","a30","a31","a32","a33","a34","a35","a36","a37","a38","a39","a40","a41","a42","a43","a44","a45","a46","a47","a48","a49","a50","a51","a52","a53","a54","a55","a56","a57","a58","a59","a60","a61","a62","a63","a64","a65","a66","a67","a68","a69","a70","a71","a72","a73","a74","a75","a76","a77","a78","a79","a80","a81","a82","a83","a84","a85","a86","a87","a88","a89","a90","a91","a92","a93","a94","a95","a96","a97","a98","a99","a100","a101","a102","a103","a104","a105","a106","a107","a108","a109","a110","a111","a112","a113","a114","a115","a116","a117","a118","a119","a120","a121","a122","a123","a124","a125","a126","a127","a128","a129","a130","a131","a132","a133","a134","a135","a136","a137","a138","a139","a140","a141","a142","a143","a144","a145","a146","a147","a148","a149","a150","a151","a152","a153","a154","a155","a156","a157","a158","a159","a160","a161","a162","a163","a164","a165","a166","a167","a168","a169","a170","a171","a172","a173","a174","a175","a176","a177","a178","a179","a180","a181","a182","a183","a184","a185","a186","a187","a188","a189","a190","a191","a192","a193","a194","a195","a196","a197","a198","a199","a200","a201","a202","a203","a204","a205","a206","a207","a208","a209","a210","a211","a212","a213","a214","a215","a216","a217","a218","a219","a220","a221","a222","a223","a224","a225","a226","a227","a228","a229","a230","a231","a232","a233","a234","a235","a236","a237","a238","a239","a240","a241","a242","a243","a244","a245","a246","a247","a248","a249","a250","a251","a252","a253","a254","a255","v1","v2","v3","v4","v5","v6","v7","v8","v9","v10","v11","v12","v13","v14","v15","v16","v17","v18","v19","v20","v21","v22","v23","v24","v25","v26","v27","v28","v29","v30","v31","v32","v33","v34","v35","v36","v37","v38","v39","v40","v41","v42","v43","v44","v45","v46","v47","v48","v49","v50","v51","v52","v53","v54","v55","v56","v57","v58","v59","v60","v61","v62","v63","v64","v65","v66","v67","v68","v69","v70","v71","v72","v73","v74","v75","v76","v77","v78","v79","v80","v81","v82","v83","v84","v85","v86","v87","v88","v89","v90","v91","v92","v93","v94","v95","v96","v97","v98","v99","v100","v101","v102","v103","v104","v105","v106","v107","v108","v109","v110","v111","v112","v113","v114","v115","v116","v117","v118","v119","v120","v121","v122","v123","v124","v125","v126","v127","v128","v129","v130","v131","v132","v133","v134","v135","v136","v137","v138","v139","v140","v141","v142","v143","v144","v145","v146","v147","v148","v149","v150","v151","v152","v153","v154","v155","v156","v157","v158","v159","v160","v161","v162","v163","v164","v165","v166","v167","v168","v169","v170","v171","v172","v173","v174","v175","v176","v177","v178","v179","v180","v181","v182","v183","v184","v185","v186","v187","v188","v189","v190","v191","v192","v193","v194","v195","v196","v197","v198","v199","v200","v201","v202","v203","v204","v205","v206","v207","v208","v209","v210","v211","v212","v213","v214","v215","v216","v217","v218","v219","v220","v221","v222","v223","v224","v225","v226","v227","v228","v229","v230","v231","v232","v233","v234","v235","v236","v237","v238","v239","v240","v241","v242","v243","v244","v245","v246","v247","v248","v249","v250","v251","v252","v253","v254","v255","s36","s37","s38","s39","s40","s41","s42","s43","s44","s45","s46","s47","s48","s49","s50","s51","s52","s53","s54","s55","s56","s57","s58","s59","s60","s61","s62","s63","s64","s65","s66","s67","s68","s69","s70","s71","s72","s73","s74","s75","s76","s77","s78","s79","s80","s81","s82","s83","s84","s85","s86","s87","s88","s89","s90","s91","s92","s93","s94","s95","s96","s97","s98","s99","scc","vcc","m0","memory");
}

extern "C" void kernel_launch(void* const* d_in, const int* in_sizes, int n_in,
                              void* d_out, int out_size, void* d_ws, size_t ws_size,
                              hipStream_t stream) {
  conv_wavernn_kernel<<<256, 256, 0, stream>>>((const char*)d_in[0], (const char*)d_in[1], (char*)d_out, (char*)d_ws);
}
```
